```python
import torch

def _softmax(x, axis=-1):
    return torch.softmax(x, dim=axis)
def _gelu(x):
    return torch.nn.functional.gelu(x, approximate="tanh")
def _silu(x):
    return torch.nn.functional.silu(x)
def _sigmoid(x):
    return torch.sigmoid(x)
def _layernorm(x, axis=-1, eps=1e-5):
    m = x.mean(dim=axis, keepdim=True)
    v = x.var(dim=axis, keepdim=True, unbiased=False)
    return (x - m) / torch.sqrt(v + eps)
def _scatter_add(tgt, idx, src, axis=0):
    return tgt.clone().index_add_(axis, idx, src)
def _segment_sum(src, seg, n):
    out = torch.zeros((n,) + src.shape[1:], dtype=src.dtype)
    return out.index_add_(0, seg, src)
def _pad(x, pw):
    flat = tuple(v for pair in reversed(pw) for v in pair)
    return torch.nn.functional.pad(x, flat)

kernel_name = 'sequential_preLN__ssm_conv_s4d__glu_gated_swiglu__prompt'

def setup_inputs():

    _w = (lambda a: (lambda t: t.to(torch.bfloat16) if t.is_floating_point() else t)(torch.as_tensor(a)))
    def _A_setup():
        Bsz, L, D, N = 1, 2048, 640, 128
        return {
            "u": _w((lambda *s: torch.randn(*s, dtype=torch.bfloat16) * 0.05)(Bsz, L, D)),
            "log_A_real": _w((lambda *s: torch.randn(*s, dtype=torch.bfloat16) * 6.4)(D, N) * 0.5),
            "A_imag": _w((lambda *s: torch.randn(*s, dtype=torch.bfloat16) * 0.05)(D, N) * 3.14),
            "C_real": _w((lambda *s: torch.randn(*s, dtype=torch.bfloat16) * 0.05)(D, N) / N ** 0.5),
            "C_imag": _w((lambda *s: torch.randn(*s, dtype=torch.bfloat16) * 0.05)(D, N) / N ** 0.5),
            "log_dt": _w((lambda *s: torch.randn(*s, dtype=torch.bfloat16) * 0.05)(D) * 0.5 - 4.0),
            "D_skip": _w((lambda *s: torch.randn(*s, dtype=torch.bfloat16) * 0.05)(D)),
        }
    _A_in = _A_setup()

    def _B_setup():
        B, S, D, F = 1, 2048, 640, 1728
        return {
            "x": _w((lambda *s: torch.randn(*s, dtype=torch.bfloat16) * 0.05)(B, S, D)),
            "w1": _w((lambda *s: torch.randn(*s, dtype=torch.bfloat16) * 0.4)(D, F) * (2.0 / D) ** 0.5),
            "w3": _w((lambda *s: torch.randn(*s, dtype=torch.bfloat16) * 0.4)(D, F) * (2.0 / D) ** 0.5),
            "w2": _w((lambda *s: torch.randn(*s, dtype=torch.bfloat16) * 0.4)(F, D) * (2.0 / F) ** 0.5),
        }
    _B_in = _B_setup()

    return {
        "A_u": _A_in["u"],
        "A_log_A_real": _A_in["log_A_real"],
        "A_A_imag": _A_in["A_imag"],
        "A_C_real": _A_in["C_real"],
        "A_C_imag": _A_in["C_imag"],
        "A_log_dt": _A_in["log_dt"],
        "A_D_skip": _A_in["D_skip"],
        "B_x": _B_in["x"],
        "B_w1": _B_in["w1"],
        "B_w3": _B_in["w3"],
        "B_w2": _B_in["w2"],
    }

def reference(A_u, A_log_A_real, A_A_imag, A_C_real, A_C_imag, A_log_dt, A_D_skip, B_x, B_w1, B_w3, B_w2):
    def _A_ref(u, log_A_real, A_imag, C_real, C_imag, log_dt, D_skip):
        L = u.shape[1]
        dt = torch.exp(log_dt)
        A = -torch.exp(log_A_real) + 1j * A_imag
        C = (C_real + 1j * C_imag) * dt[:, None]
        pos = (lambda x, dt: x.to(dt))(torch.arange(L), log_dt.dtype)
        P = torch.exp((A * dt[:, None])[:, :, None] * pos[None, None, :])
        K = 2.0 * torch.einsum("dn,dnl->dl", C, P).real
        L2 = 2 * L
        u_t = (lambda x, *d: x.permute(*d))(u, 0, 2, 1)
        Kf = (lambda x, *a, **k: torch.fft.rfft(x.float(), *a, **k))(K, L2)
        Uf = (lambda x, *a, **k: torch.fft.rfft(x.float(), *a, **k))(u_t, L2)
        y = (lambda x, *a, **k: torch.fft.irfft(x, *a, **k).to(torch.bfloat16))(Uf * Kf[None, :, :], L2)[:, :, :L]
        y = (lambda x, *d: x.permute(*d))(y, 0, 2, 1)
        return y + u * D_skip[None, None, :]

    def _B_ref(x, w1, w3, w2):
        gate = torch.einsum("bsd,df->bsf", x, w1)
        up = torch.einsum("bsd,df->bsf", x, w3)
        h = _silu(gate) * up
        out = torch.einsum("bsf,fd->bsd", h, w2)
        return out

    x = A_u
    h = x + _A_ref(_layernorm(x, axis=-1), A_log_A_real, A_A_imag, A_C_real, A_C_imag, A_log_dt, A_D_skip)
    h2 = h + _B_ref(_layernorm(h, axis=-1), B_w1, B_w3, B_w2)
    return h2
```

```cpp
#include <hip/hip_runtime.h>
#include <hip/hip_bf16.h>
#include <stdint.h>

#define GAS __attribute__((address_space(1)))
#define LAS __attribute__((address_space(3)))
#define DI __device__ __forceinline__

typedef unsigned short u16;
typedef __bf16 bf16x8 __attribute__((ext_vector_type(8)));
typedef __bf16 bf16x2 __attribute__((ext_vector_type(2)));
typedef _Float16 f16x8 __attribute__((ext_vector_type(8)));
typedef _Float16 f16x2 __attribute__((ext_vector_type(2)));
typedef short s16x4 __attribute__((ext_vector_type(4)));
typedef float f32x4 __attribute__((ext_vector_type(4)));
typedef float f32x2 __attribute__((ext_vector_type(2)));
typedef unsigned v4u __attribute__((ext_vector_type(4)));
typedef unsigned v2u __attribute__((ext_vector_type(2)));
typedef GAS unsigned gu32;
typedef GAS unsigned long long gu64;

constexpr int L = 2048, D = 640, NM = 128, F = 1728;

constexpr size_t WS_GEN = 0;
constexpr size_t WS_STATS = 4096;
constexpr size_t WS_CS = 32768;
constexpr size_t WS_HT = 65536;
constexpr size_t WS_W13 = WS_HT + (size_t)D * L * 2;
constexpr size_t WS_W2 = WS_W13 + (size_t)108 * 2 * 20 * 1024;
constexpr size_t WS_HM = WS_W2 + (size_t)40 * 54 * 1024;
constexpr size_t WS_END = WS_HM + (size_t)128 * 54 * 1024;

struct Params {
  const u16* x;
  const u16* logA;
  const u16* Aim;
  const u16* Cre;
  const u16* Cim;
  const u16* logdt;
  const u16* Dsk;
  const u16* w1;
  const u16* w3;
  const u16* w2;
  u16* out;
  unsigned char* ws;
};

DI float bf2f(unsigned b) { return __builtin_bit_cast(float, b << 16); }
DI float bflo(unsigned v) { return __builtin_bit_cast(float, v << 16); }
DI float bfhi(unsigned v) { return __builtin_bit_cast(float, v & 0xffff0000u); }
DI unsigned pkbf(float a, float b) {
  bf16x2 r = {(__bf16)a, (__bf16)b};
  return __builtin_bit_cast(unsigned, r);
}
DI unsigned pkh(float a, float b) {
  f16x2 r = {(_Float16)a, (_Float16)b};
  return __builtin_bit_cast(unsigned, r);
}
DI float rbf(float a) { return bflo(pkbf(a, a)); }
DI float hlo(unsigned v) { return (float)__builtin_bit_cast(f16x2, v)[0]; }
DI float hhi(unsigned v) { return (float)__builtin_bit_cast(f16x2, v)[1]; }

template <int CTRL> DI float dppf(float v) {
  return __builtin_bit_cast(float, __builtin_amdgcn_update_dpp(0, __builtin_bit_cast(int, v), CTRL, 0xf, 0xf, true));
}
DI float xrow16(float v) {
  unsigned u = __builtin_bit_cast(unsigned, v);
  auto a = __builtin_amdgcn_permlane16_swap(u, u, false, false);
  v = __builtin_bit_cast(float, (unsigned)a[0]) + __builtin_bit_cast(float, (unsigned)a[1]);
  u = __builtin_bit_cast(unsigned, v);
  auto b = __builtin_amdgcn_permlane32_swap(u, u, false, false);
  return __builtin_bit_cast(float, (unsigned)b[0]) + __builtin_bit_cast(float, (unsigned)b[1]);
}
DI float wave_sum(float v) {
  v += dppf<0x128>(v);
  v += dppf<0x124>(v);
  v += dppf<0x122>(v);
  v += dppf<0x121>(v);
  return xrow16(v);
}
DI float wave_max(float v) {
  v = fmaxf(v, dppf<0x128>(v));
  v = fmaxf(v, dppf<0x124>(v));
  v = fmaxf(v, dppf<0x122>(v));
  v = fmaxf(v, dppf<0x121>(v));
  unsigned u = __builtin_bit_cast(unsigned, v);
  auto a = __builtin_amdgcn_permlane16_swap(u, u, false, false);
  v = fmaxf(__builtin_bit_cast(float, (unsigned)a[0]), __builtin_bit_cast(float, (unsigned)a[1]));
  u = __builtin_bit_cast(unsigned, v);
  auto b = __builtin_amdgcn_permlane32_swap(u, u, false, false);
  return fmaxf(__builtin_bit_cast(float, (unsigned)b[0]), __builtin_bit_cast(float, (unsigned)b[1]));
}
DI f32x4 mfma_bf(v4u a, v4u b, f32x4 c) {
  return __builtin_amdgcn_mfma_f32_16x16x32_bf16(__builtin_bit_cast(bf16x8, a), __builtin_bit_cast(bf16x8, b), c, 0, 0, 0);
}
DI f32x4 mfma_h(v4u a, v4u b, f32x4 c) {
  return __builtin_amdgcn_mfma_f32_16x16x32_f16(__builtin_bit_cast(f16x8, a), __builtin_bit_cast(f16x8, b), c, 0, 0, 0);
}
DI v2u trread(LAS char* p) {
  s16x4 v = __builtin_amdgcn_ds_read_tr16_b64_v4i16((LAS s16x4*)p);
  return __builtin_bit_cast(v2u, v);
}
DI v4u ldsr128(LAS char* p) { return *(LAS v4u*)p; }
DI v2u ldsr64(LAS char* p) { return *(LAS v2u*)p; }
DI void glds16(const void* g, LAS char* l) {
  __builtin_amdgcn_global_load_lds((const GAS void*)g, (LAS void*)l, 16, 0, 0);
}
#define RLX_AGENT __ATOMIC_RELAXED, __HIP_MEMORY_SCOPE_AGENT
#define WAITV(n) asm volatile("s_waitcnt vmcnt(" #n ")" ::: "memory")
#define BAR() do { asm volatile("s_waitcnt lgkmcnt(0)" ::: "memory"); __builtin_amdgcn_s_barrier(); asm volatile("" ::: "memory"); } while (0)

struct cplx { float re, im; };
DI cplx cmul(cplx a, cplx b) { return {a.re * b.re - a.im * b.im, a.re * b.im + a.im * b.re}; }

constexpr int KG_ROW = 272;
constexpr int KG_GA = 21 * KG_ROW;
constexpr int KG_WAVE = KG_GA + 32 * KG_ROW;
constexpr int A_KG = 0;
constexpr int KC_COPY = 4416, KC_CH = 4 * KC_COPY;
constexpr int A_EX = 4 * KC_CH;
constexpr int A_U = 115328;
constexpr int U_CH = 4608;
constexpr int A_X = A_U + 4 * U_CH;
constexpr int A_T = A_X + 4 * 4096;
constexpr int T_CH = 1344;
constexpr int A_SC = A_T + 4 * T_CH;
constexpr int A_LDS = A_SC + 64;
static_assert(A_EX + 8 * 4096 <= A_U, "lds map");
static_assert(A_LDS <= 163840, "lds");

DI int tab_idx(int l) {
  unsigned r = pkbf((float)l, 0.f) & 0xffffu;
  int i = (int)r - 0x4300 + 128;
  return l < 128 ? l : i;
}

DI void conv_role(const Params& p, LAS char* sm, int tid, int lane, int w, int cgp, unsigned tag) {
  const int c0 = 4 * cgp;
  const int kch = c0 + (w >> 1), hf = w & 1, n = 64 * hf + lane;
  const unsigned vla = p.logA[kch * NM + n], vai = p.Aim[kch * NM + n];
  const unsigned vcr = p.Cre[kch * NM + n], vci = p.Cim[kch * NM + n];
  const unsigned vcr2 = p.Cre[kch * NM + (n ^ 64)], vci2 = p.Cim[kch * NM + (n ^ 64)];
  const unsigned vdt = p.logdt[kch];
  v2u xg[4];
#pragma unroll
  for (int k = 0; k < 4; ++k) xg[k] = *(const GAS v2u*)(p.x + (size_t)(4 * tid + k) * D + c0);

  const int ec = tid & 255, epair = tid >> 8;
  int eidx[11];
#pragma unroll
  for (int i = 0; i < 11; ++i) eidx[i] = 2 * tab_idx(2051 - 8 * ec - i);

  {
    const float dt = rbf(expf(bf2f(vdt)));
    const float are = -rbf(expf(bf2f(vla))) * dt, aim = bf2f(vai) * dt;
    float cmax = fmaxf(fmaxf(fabsf(bf2f(vcr)), fabsf(bf2f(vci))), fmaxf(fabsf(bf2f(vcr2)), fabsf(bf2f(vci2))));
    cmax = wave_max(cmax);
    const float cinv = cmax > 0.f ? 1.0f / cmax : 0.f;
    if (hf == 0 && lane == 0) *(LAS float*)(sm + A_SC + 4 * (w >> 1)) = 2.0f * dt * cmax;
    const float al = are * 1.4426950408889634f, ar = aim * 0.15915494309189535f;
    auto cpow = [&](float pw) -> cplx {
      const float e = __builtin_amdgcn_exp2f(al * pw);
      const float f = __builtin_amdgcn_fractf(ar * pw);
      return {e * __builtin_amdgcn_cosf(f), e * __builtin_amdgcn_sinf(f)};
    };
    const cplx w1 = cpow(1.f), w8 = cpow(8.f), w16 = cpow(16.f), W1 = cpow(256.f);
    LAS char* ga = sm + A_KG + w * KG_WAVE + 4 * lane;
    LAS char* eb = ga + KG_GA;
    cplx e0 = {1.f, 0.f}, e1 = w8;
#pragma unroll
    for (int j = 0; j < 32; j += 2) {
      *(LAS unsigned*)(eb + j * KG_ROW) = pkh(e0.re, e0.im);
      *(LAS unsigned*)(eb + (j + 1) * KG_ROW) = pkh(e1.re, e1.im);
      if (j + 2 < 32) { e0 = cmul(e0, w16); e1 = cmul(e1, w16); }
    }
    auto putg = [&](int row, cplx g) { *(LAS unsigned*)(ga + row * KG_ROW) = pkh(g.re, -g.im); };
    cplx g[8];
    g[0] = {bf2f(vcr) * cinv, bf2f(vci) * cinv};
#pragma unroll
    for (int r = 1; r < 8; ++r) g[r] = cmul(g[r - 1], w1);
#pragma unroll
    for (int r = 0; r < 8; ++r) putg(r, g[r]);
    const cplx W2 = cmul(W1, W1), W3 = cmul(W2, W1), W4 = cmul(W2, W2);
#pragma unroll
    for (int bb = 0; bb < 4; ++bb) putg(8 + bb, cmul(g[2 * bb], W1));
    putg(12, cmul(g[0], W2));
    putg(13, cmul(g[4], W2));
    putg(14, cmul(g[0], W3));
    putg(15, cmul(g[4], W3));
    const cplx G4 = cmul(g[0], W4);
    putg(16, G4);
    putg(17, cmul(G4, W1));
    putg(18, cmul(G4, W2));
    putg(19, cmul(G4, W3));
    putg(20, cmul(G4, W4));
  }
  BAR();
  {
    const int r = lane & 15, q = lane >> 4;
    f32x4 d0 = {0, 0, 0, 0}, d1 = {0, 0, 0, 0};
#pragma unroll
    for (int s = 0; s < 8; ++s) {
      LAS char* img = sm + A_KG + ((w & ~1) + (s >> 2)) * KG_WAVE + (s & 3) * 64 + 16 * q;
      const v4u a0 = ldsr128(img + r * KG_ROW);
      const v4u a1 = ldsr128(img + (16 + r) * KG_ROW);
      const v4u bb = ldsr128(img + KG_GA + (16 * hf + r) * KG_ROW);
      d0 = mfma_h(a0, bb, d0);
      d1 = mfma_h(a1, bb, d1);
    }
    const int j = 16 * hf + r;
    LAS char* T = sm + A_T + (w >> 1) * T_CH;
#pragma unroll
    for (int i = 0; i < 4; ++i) {
      int i0 = q < 2 ? 4 * q + i + 8 * j : (q == 2 ? 256 + i + 4 * j : 384 + (i & 1) + 64 * (i >> 1) + 2 * j);
      *(LAS _Float16*)(T + 2 * i0) = (_Float16)d0[i];
      if (q == 0) *(LAS _Float16*)(T + 2 * (512 + 32 * i + j)) = (_Float16)d1[i];
    }
    if (q == 1 && j == 0) *(LAS _Float16*)(T + 2 * 640) = (_Float16)d1[0];
  }
  {
    const gu64* gr = (const gu64*)(p.ws + WS_STATS) + 4 * tid;
    unsigned st[4];
    for (unsigned spin = 0; spin < (1u << 20); ++spin) {
      bool ok = true;
#pragma unroll
      for (int k = 0; k < 4; ++k) {
        const unsigned long long g = __hip_atomic_load(gr + k, RLX_AGENT);
        st[k] = (unsigned)g;
        ok &= (unsigned)(g >> 32) == tag;
      }
      if (__all(ok)) break;
      __builtin_amdgcn_s_sleep(1);
    }
    float uu[4][4];
#pragma unroll
    for (int k = 0; k < 4; ++k) {
      const float m = bflo(st[k]), rs = 1.0f / bfhi(st[k]);
      uu[k][0] = rbf(rbf(bflo(xg[k].x) - m) * rs);
      uu[k][1] = rbf(rbf(bfhi(xg[k].x) - m) * rs);
      uu[k][2] = rbf(rbf(bflo(xg[k].y) - m) * rs);
      uu[k][3] = rbf(rbf(bfhi(xg[k].y) - m) * rs);
    }
#pragma unroll
    for (int ch = 0; ch < 4; ++ch) {
      v2u o = {pkh(uu[0][ch], uu[1][ch]), pkh(uu[2][ch], uu[3][ch])};
      *(LAS v2u*)(sm + A_U + ch * U_CH + 512 + 8 * tid) = o;
    }
    const unsigned s0 = 0x05040100u, s1 = 0x07060302u;
    v2u x0 = {__builtin_amdgcn_perm(xg[1].x, xg[0].x, s0), __builtin_amdgcn_perm(xg[3].x, xg[2].x, s0)};
    v2u x1 = {__builtin_amdgcn_perm(xg[1].x, xg[0].x, s1), __builtin_amdgcn_perm(xg[3].x, xg[2].x, s1)};
    v2u x2 = {__builtin_amdgcn_perm(xg[1].y, xg[0].y, s0), __builtin_amdgcn_perm(xg[3].y, xg[2].y, s0)};
    v2u x3 = {__builtin_amdgcn_perm(xg[1].y, xg[0].y, s1), __builtin_amdgcn_perm(xg[3].y, xg[2].y, s1)};
    *(LAS v2u*)(sm + A_X + 0 * 4096 + 8 * tid) = x0;
    *(LAS v2u*)(sm + A_X + 1 * 4096 + 8 * tid) = x1;
    *(LAS v2u*)(sm + A_X + 2 * 4096 + 8 * tid) = x2;
    *(LAS v2u*)(sm + A_X + 3 * 4096 + 8 * tid) = x3;
    if (tid < 128) {
      v4u z = {0, 0, 0, 0};
      *(LAS v4u*)(sm + A_U + (tid >> 5) * U_CH + 16 * (tid & 31)) = z;
    }
  }
  BAR();
  if (cgp == 0 && tid == 0) __hip_atomic_store((gu32*)(p.ws + WS_GEN), tag, RLX_AGENT);
  {
#pragma unroll
    for (int cc = 0; cc < 2; ++cc) {
      const int ch = 2 * epair + cc;
      LAS char* T = sm + A_T + ch * T_CH;
      unsigned v[11];
#pragma unroll
      for (int i = 0; i < 11; ++i) v[i] = *(LAS u16*)(T + eidx[i]);
      unsigned d[6];
#pragma unroll
      for (int i = 0; i < 5; ++i) d[i] = v[2 * i] | (v[2 * i + 1] << 16);
      d[5] = v[10];
      LAS char* K = sm + A_KG + ch * KC_CH + 16 * ec;
      v4u c3 = {d[0], d[1], d[2], d[3]};
      v4u c1 = {d[1], d[2], d[3], d[4]};
      v4u c2, c0v;
#pragma unroll
      for (int i = 0; i < 4; ++i) {
        c2[i] = __builtin_amdgcn_alignbit(d[i + 1], d[i], 16);
        c0v[i] = __builtin_amdgcn_alignbit(d[i + 2], d[i + 1], 16);
      }
      *(LAS v4u*)(K + 0 * KC_COPY) = c0v;
      *(LAS v4u*)(K + 1 * KC_COPY) = c1;
      *(LAS v4u*)(K + 2 * KC_COPY) = c2;
      *(LAS v4u*)(K + 3 * KC_COPY) = c3;
    }
    if (tid < 256) {
      const int ch = tid >> 6, pp = (tid >> 4) & 3, i = tid & 15;
      const int l = pp - i;
      const u16 val = l >= 0 ? *(LAS u16*)(sm + A_T + ch * T_CH + 2 * l) : (u16)0;
      *(LAS u16*)(sm + A_KG + ch * KC_CH + pp * KC_COPY + 2 * (2048 + i)) = val;
    }
  }
  BAR();
  const int ch = w & 3, half = w >> 2, r = lane & 15, q = lane >> 4;
  f32x4 acc[8];
#pragma unroll
  for (int i = 0; i < 8; ++i) acc[i] = (f32x4){0, 0, 0, 0};
  {
    LAS char* abase = sm + A_KG + ch * KC_CH + (r & 3) * KC_COPY + 2 * (2048 - 16 - 4 * (r >> 2) + 8 * q) - 512 * 7;
    LAS char* bbase = sm + A_U + ch * U_CH + 512 + 32 * r - 32 + 16 * q;
#pragma unroll
    for (int dd = 0; dd < 4; ++dd) {
      const int dp = 4 * half + dd;
      LAS char* ap = abase - 64 * dp;
      LAS char* bp = bbase - 64 * dp;
      v4u a[8], bfr[8];
#pragma unroll
      for (int g = 0; g < 8; ++g) {
        const v2u lo = ldsr64(ap + 512 * (7 - g)), hi = ldsr64(ap + 512 * (7 - g) + 8);
        a[g] = (v4u){lo.x, lo.y, hi.x, hi.y};
        bfr[g] = ldsr128(bp + 512 * g);
      }
#pragma unroll
      for (int g = 0; g < 8; ++g)
#pragma unroll
        for (int nn = g; nn < 8; ++nn) acc[nn] = mfma_h(a[g], bfr[nn - g], acc[nn]);
    }
  }
  {
    LAS char* mine = sm + A_EX + w * 4096 + 16 * lane;
    LAS char* other = sm + A_EX + (w ^ 4) * 4096 + 16 * lane;
    f32x4 keep[4];
    if (half == 0) {
#pragma unroll
      for (int i = 0; i < 4; ++i) { *(LAS f32x4*)(mine + 1024 * i) = acc[4 + i]; keep[i] = acc[i]; }
    } else {
#pragma unroll
      for (int i = 0; i < 4; ++i) { *(LAS f32x4*)(mine + 1024 * i) = acc[i]; keep[i] = acc[4 + i]; }
    }
    BAR();
    const float scale = *(LAS float*)(sm + A_SC + 4 * ch);
    const float dsk = bf2f(p.Dsk[c0 + ch]);
#pragma unroll
    for (int i = 0; i < 4; ++i) {
      const f32x4 y = keep[i] + *(LAS f32x4*)(other + 1024 * i);
      const int t0 = 256 * (4 * half + i) + 16 * r + 4 * q;
      const v2u uv = ldsr64(sm + A_U + ch * U_CH + 512 + 2 * t0);
      LAS char* xp = sm + A_X + ch * 4096 + 2 * t0;
      const v2u xv = ldsr64(xp);
      const float u4[4] = {hlo(uv.x), hhi(uv.x), hlo(uv.y), hhi(uv.y)};
      const float x4[4] = {bflo(xv.x), bfhi(xv.x), bflo(xv.y), bfhi(xv.y)};
      float h4[4];
#pragma unroll
      for (int e = 0; e < 4; ++e) h4[e] = x4[e] + rbf(rbf(y[e] * scale) + rbf(u4[e] * dsk));
      *(LAS v2u*)xp = (v2u){pkbf(h4[0], h4[1]), pkbf(h4[2], h4[3])};
    }
    u16* hT = (u16*)(p.ws + WS_HT) + (size_t)(c0 + ch) * L + 1024 * half;
#pragma unroll
    for (int i = 0; i < 2; ++i) {
      const v4u v = ldsr128(sm + A_X + ch * 4096 + 2048 * half + 1024 * i + 16 * lane);
      *(GAS v4u*)(hT + 512 * i + 8 * lane) = v;
    }
  }
}

DI void helper_role(const Params& p, LAS char* sm, int lane, int w, int hidx) {
  const int g = lane >> 4, qq = (lane & 15) >> 2, pp = lane & 3;
  if (hidx < 54) {
    const int mat = hidx / 27, cb = hidx % 27;
    const u16* W = mat ? p.w3 : p.w1;
    LAS char* buf = sm + w * 12288;
#pragma unroll
    for (int i = 0; i < 3; ++i) {
      const int s = w + 8 * i;
      if (s < 20) {
#pragma unroll
        for (int k = 0; k < 4; ++k)
          glds16(W + (size_t)(32 * s + 8 * k + (lane >> 3)) * F + 64 * cb + 8 * (lane & 7), buf + i * 4096 + k * 1024);
      }
    }
    WAITV(0);
    float cs[4] = {0, 0, 0, 0};
#pragma unroll
    for (int i = 0; i < 3; ++i) {
      const int s = w + 8 * i;
      if (s < 20) {
#pragma unroll
        for (int u = 0; u < 4; ++u) {
          LAS char* a = buf + i * 4096 + (8 * g + qq) * 128 + u * 32 + pp * 8;
          const v2u lo = trread(a), hi = trread(a + 4 * 128);
          const v4u fr = {lo.x, lo.y, hi.x, hi.y};
          *(GAS v4u*)(p.ws + WS_W13 + ((size_t)((4 * cb + u) * 2 + mat) * 20 + s) * 1024 + 16 * lane) = fr;
          cs[u] += (bflo(fr.x) + bfhi(fr.x)) + (bflo(fr.y) + bfhi(fr.y)) + (bflo(fr.z) + bfhi(fr.z)) + (bflo(fr.w) + bfhi(fr.w));
        }
      }
    }
    LAS float* red = (LAS float*)(sm + 98304);
#pragma unroll
    for (int u = 0; u < 4; ++u) {
      const float t = xrow16(cs[u]);
      if (lane < 16) red[w * 64 + u * 16 + lane] = t;
    }
    __syncthreads();
    if (w == 0) {
      float t = 0;
#pragma unroll
      for (int i = 0; i < 8; ++i) t += red[i * 64 + lane];
      ((float*)(p.ws + WS_CS))[mat * F + 64 * cb + lane] = t;
    }
  } else {
    LAS char* buf = sm + w * 8192;
#pragma unroll
    for (int i = 0; i < 2; ++i) {
      const int blk = (hidx - 54) * 8 + w + 336 * i;
      if (blk < 540) {
        const int s = blk / 10, cb = blk % 10;
#pragma unroll
        for (int k = 0; k < 4; ++k)
          glds16(p.w2 + (size_t)(32 * s + 8 * k + (lane >> 3)) * D + 64 * cb + 8 * (lane & 7), buf + i * 4096 + k * 1024);
      }
    }
    WAITV(0);
#pragma unroll
    for (int i = 0; i < 2; ++i) {
      const int blk = (hidx - 54) * 8 + w + 336 * i;
      if (blk < 540) {
        const int s = blk / 10, cb = blk % 10;
#pragma unroll
        for (int u = 0; u < 4; ++u) {
          LAS char* a = buf + i * 4096 + (8 * g + qq) * 128 + u * 32 + pp * 8;
          const v2u lo = trread(a), hi = trread(a + 4 * 128);
          const v4u fr = {lo.x, lo.y, hi.x, hi.y};
          *(GAS v4u*)(p.ws + WS_W2 + ((size_t)(4 * cb + u) * 54 + s) * 1024 + 16 * lane) = fr;
        }
      }
    }
  }
}

__global__ __launch_bounds__(512, 2) void kA(Params p) {
  extern __shared__ __attribute__((aligned(16))) char smem[];
  LAS char* sm = (LAS char*)smem;
  const int tid = threadIdx.x, lane = tid & 63, w = __builtin_amdgcn_readfirstlane(tid >> 6);
  const int b = blockIdx.x, xl = b & 7, j = b >> 3;
  const unsigned tag = __hip_atomic_load((gu32*)(p.ws + WS_GEN), RLX_AGENT) + 1u;
  {
    const int row = 8 * b + w;
    unsigned xr[5];
#pragma unroll
    for (int i = 0; i < 5; ++i) xr[i] = *(const GAS unsigned*)(p.x + (size_t)row * D + 2 * (lane + 64 * i));
    float s = 0;
#pragma unroll
    for (int i = 0; i < 5; ++i) s += bflo(xr[i]) + bfhi(xr[i]);
    const float mean = wave_sum(s) * (1.0f / D);
    float v = 0;
#pragma unroll
    for (int i = 0; i < 5; ++i) {
      const float a = bflo(xr[i]) - mean, c = bfhi(xr[i]) - mean;
      v += a * a + c * c;
    }
    const float var = wave_sum(v) * (1.0f / D);
    const float sd = rbf(sqrtf(rbf(rbf(var) + 1e-5f)));
    const unsigned pay = (pkbf(mean, sd));
    if (lane == 0)
      __hip_atomic_store((gu64*)(p.ws + WS_STATS) + row, ((unsigned long long)tag << 32) | pay, RLX_AGENT);
  }
  if (j < 20) conv_role(p, sm, tid, lane, w, 20 * xl + j, tag);
  else helper_role(p, sm, lane, w, (j - 20) * 8 + xl);
}

constexpr int G1_STAGE = 16384 + 28 * 1024;
constexpr int G1_RED = 3 * G1_STAGE;
constexpr int G1_LDS = G1_RED + 1024;

__global__ __launch_bounds__(512, 2) void kG1(Params p) {
  extern __shared__ __attribute__((aligned(16))) char smem[];
  LAS char* sm = (LAS char*)smem;
  const int tid = threadIdx.x, lane = tid & 63, w = __builtin_amdgcn_readfirstlane(tid >> 6);
  const int b = blockIdx.x, xl = b & 7, j = b >> 3;
  const int rgi = j & 7, cgi = j >> 3;
  const int rg = 8 * (xl & 1) + rgi, cg = 4 * (xl >> 1) + cgi;
  const int u0 = cg < 12 ? 7 * cg : 84 + 6 * (cg - 12), nu = cg < 12 ? 7 : 6;
  const int tw = w & 3, uh = w >> 2;
  const int kt0 = (rgi + 3 * cgi) % 10;

  const u16* hT = (const u16*)(p.ws + WS_HT) + 128 * rg;
  const unsigned char* W = p.ws + WS_W13;
  const int hrow = lane >> 4;
  auto issue = [&](int it) {
    int kt = kt0 + it; kt -= kt >= 10 ? 10 : 0;
    LAS char* st = sm + (it % 3) * G1_STAGE;
    if (uh == 0) {
#pragma unroll
      for (int i = 0; i < 4; ++i) {
        const int row = 16 * tw + 4 * i + hrow;
        const int ch = (lane & 15) ^ (((row & 3) << 2) | ((row >> 2) & 3));
        glds16(hT + (size_t)(64 * kt + row) * L + 8 * ch, st + (16 * tw + 4 * i) * 256);
      }
    } else {
#pragma unroll
      for (int i = 0; i < 7; ++i) {
        const int fi = 7 * tw + i;
        int un = fi >> 2; un = un < nu ? un : nu - 1;
        const int mat = (fi >> 1) & 1, ks = fi & 1;
        glds16(W + ((size_t)((u0 + un) * 2 + mat) * 20 + 2 * kt + ks) * 1024 + 16 * lane, st + 16384 + fi * 1024);
      }
    }
  };
  f32x4 ag[4][2], au[4][2];
#pragma unroll
  for (int i = 0; i < 4; ++i)
#pragma unroll
    for (int t = 0; t < 2; ++t) { ag[i][t] = (f32x4){0, 0, 0, 0}; au[i][t] = (f32x4){0, 0, 0, 0}; }
  float ssum[2] = {0, 0}, ssq[2] = {0, 0};

  issue(0);
  issue(1);
  const int g = lane >> 4, qq = (lane & 15) >> 2, pp = lane & 3;
  for (int it = 0; it < 10; ++it) {
    if (it < 9) { if (uh == 0) WAITV(4); else WAITV(7); } else WAITV(0);
    BAR();
    if (it + 2 < 10) issue(it + 2);
    LAS char* st = sm + (it % 3) * G1_STAGE;
#pragma unroll
    for (int ks = 0; ks < 2; ++ks) {
      v4u bfr[2];
#pragma unroll
      for (int t = 0; t < 2; ++t) {
        v2u h2[2];
#pragma unroll
        for (int tt = 0; tt < 2; ++tt) {
          const int row = 32 * ks + 8 * g + 4 * tt + qq;
          const int ch = (2 * (2 * tw + t) + (pp >> 1)) ^ (((row & 3) << 2) | ((row >> 2) & 3));
          h2[tt] = trread(st + row * 256 + 16 * ch + 8 * (pp & 1));
        }
        bfr[t] = (v4u){h2[0].x, h2[0].y, h2[1].x, h2[1].y};
      }
      if (uh == 0) {
#pragma unroll
        for (int i = 0; i < 4; ++i) {
          const v4u fg = ldsr128(st + 16384 + (4 * i + ks) * 1024 + 16 * lane);
          const v4u fu = ldsr128(st + 16384 + (4 * i + 2 + ks) * 1024 + 16 * lane);
#pragma unroll
          for (int t = 0; t < 2; ++t) { ag[i][t] = mfma_bf(fg, bfr[t], ag[i][t]); au[i][t] = mfma_bf(fu, bfr[t], au[i][t]); }
        }
      } else {
#pragma unroll
        for (int i = 0; i < 3; ++i) {
          const v4u fg = ldsr128(st + 16384 + (4 * (4 + i) + ks) * 1024 + 16 * lane);
          const v4u fu = ldsr128(st + 16384 + (4 * (4 + i) + 2 + ks) * 1024 + 16 * lane);
#pragma unroll
          for (int t = 0; t < 2; ++t) { ag[i][t] = mfma_bf(fg, bfr[t], ag[i][t]); au[i][t] = mfma_bf(fu, bfr[t], au[i][t]); }
        }
#pragma unroll
        for (int t = 0; t < 2; ++t)
#pragma unroll
          for (int e = 0; e < 4; ++e) {
            const unsigned hw = bfr[t][e];
            const bf16x2 hv = __builtin_bit_cast(bf16x2, hw);
            ssq[t] = __builtin_amdgcn_fdot2_f32_bf16(hv, hv, ssq[t], false);
            ssum[t] = __builtin_amdgcn_fdot2_f32_bf16(hv, __builtin_bit_cast(bf16x2, 0x3f803f80u), ssum[t], false);
          }
      }
    }
  }
  LAS float* red = (LAS float*)(sm + G1_RED);
  const int r = lane & 15;
  if (uh == 1) {
#pragma unroll
    for (int t = 0; t < 2; ++t) {
      const float mean = xrow16(ssum[t]) * (1.0f / D);
      const float var = fmaxf(xrow16(ssq[t]) * (1.0f / D) - mean * mean, 0.f);
      const float sd = rbf(sqrtf(rbf(rbf(var) + 1e-5f)));
      if (g == 0) { red[2 * (32 * tw + 16 * t + r)] = rbf(mean); red[2 * (32 * tw + 16 * t + r) + 1] = 1.0f / sd; }
    }
  }
  BAR();
  const float* cs = (const float*)(p.ws + WS_CS);
  const int nun = uh == 0 ? 4 : nu - 4;
  unsigned char* HM = p.ws + WS_HM;
#pragma unroll
  for (int i = 0; i < 4; ++i) {
    if (i < nun) {
      const int un = u0 + 4 * uh + i;
      const f32x4 c1 = *(const GAS f32x4*)(cs + 16 * un + 4 * g), c3 = *(const GAS f32x4*)(cs + F + 16 * un + 4 * g);
      v2u o[2];
#pragma unroll
      for (int t = 0; t < 2; ++t) {
        const float m = red[2 * (32 * tw + 16 * t + r)], rs = red[2 * (32 * tw + 16 * t + r) + 1];
        float hv[4];
#pragma unroll
        for (int e = 0; e < 4; ++e) {
          const float gt = rbf((ag[i][t][e] - m * c1[e]) * rs);
          const float up = rbf((au[i][t][e] - m * c3[e]) * rs);
          const float sl = rbf(gt / (1.0f + __expf(-gt)));
          hv[e] = sl * up;
        }
        o[t] = (v2u){pkbf(hv[0], hv[1]), pkbf(hv[2], hv[3])};
      }
      auto sx = __builtin_amdgcn_permlane16_swap(o[0].x, o[1].x, false, false);
      auto sy = __builtin_amdgcn_permlane16_swap(o[0].y, o[1].y, false, false);
      const v4u ov = {(unsigned)sx[0], (unsigned)sy[0], (unsigned)sx[1], (unsigned)sy[1]};
      const int ttile = 8 * rg + 2 * tw + (g & 1), chunk = 2 * (un & 1) + (g >> 1);
      *(GAS v4u*)(HM + ((size_t)ttile * 54 + (un >> 1)) * 1024 + chunk * 256 + 16 * r) = ov;
    }
  }
}

constexpr int G2_STAGE = 18 * 1024;
constexpr int G2_NST = 8;
constexpr int G2_H = G2_NST * G2_STAGE;
constexpr int G2_LDS = G2_H + 80 * 128;

__global__ __launch_bounds__(512, 2) void kG2(Params p) {
  extern __shared__ __attribute__((aligned(16))) char smem[];
  LAS char* sm = (LAS char*)smem;
  const int tid = threadIdx.x, lane = tid & 63, w = __builtin_amdgcn_readfirstlane(tid >> 6);
  const int b = blockIdx.x, xl = b & 7, j = b >> 3;
  const int rgi = j & 7, dgi = j >> 3, xq = xl >> 1;
  const int rg = 8 * (xl & 1) + rgi, th = xq & 1, dg = 4 * (xq >> 1) + dgi;
  const int t0 = 128 * rg + 64 * th, d0 = 80 * dg;
  const int tt = w & 3, dh = w >> 2;
  const int kt0 = (3 * rgi + 7 * dgi) % 27;
  const unsigned char* HM = p.ws + WS_HM + (size_t)(t0 >> 4) * 54 * 1024;
  const unsigned char* W = p.ws + WS_W2 + (size_t)(5 * dg) * 54 * 1024;
  auto issue = [&](int it) {
    int kt = kt0 + it; kt -= kt >= 27 ? 27 : 0;
    LAS char* st = sm + (it % G2_NST) * G2_STAGE;
    if (dh == 0) {
#pragma unroll
      for (int ks = 0; ks < 2; ++ks)
        glds16(HM + ((size_t)tt * 54 + 2 * kt + ks) * 1024 + 16 * lane, st + (2 * tt + ks) * 1024);
    } else {
#pragma unroll
      for (int i = 0; i < 3; ++i) {
        const int fi = tt < 2 ? 3 * tt + i : 6 + 2 * (tt - 2) + i;
        if (tt < 2 || i < 2) glds16(W + ((size_t)(fi >> 1) * 54 + 2 * kt + (fi & 1)) * 1024 + 16 * lane, st + 8192 + fi * 1024);
      }
    }
  };
  {
    const u16* hT = (const u16*)(p.ws + WS_HT);
    for (int i = w; i < 10; i += 8)
      glds16(hT + (size_t)(d0 + 8 * i + (lane >> 3)) * L + t0 + 8 * (lane & 7), sm + G2_H + i * 1024);
  }
#pragma unroll
  for (int i = 0; i < G2_NST - 1; ++i) issue(i);
  f32x4 acc[3];
#pragma unroll
  for (int i = 0; i < 3; ++i) acc[i] = (f32x4){0, 0, 0, 0};
  for (int it = 0; it < 27; ++it) {
    if (it + G2_NST - 1 <= 27) {
      if (dh == 0 || tt >= 2) WAITV(12); else WAITV(18);
    } else WAITV(0);
    BAR();
    if (it + G2_NST - 1 < 27) issue(it + G2_NST - 1);
    LAS char* st = sm + (it % G2_NST) * G2_STAGE;
#pragma unroll
    for (int ks = 0; ks < 2; ++ks) {
      const v4u bfr = ldsr128(st + (2 * tt + ks) * 1024 + 16 * lane);
      if (dh == 0) {
#pragma unroll
        for (int i = 0; i < 3; ++i) acc[i] = mfma_bf(ldsr128(st + 8192 + (2 * i + ks) * 1024 + 16 * lane), bfr, acc[i]);
      } else {
#pragma unroll
        for (int i = 0; i < 2; ++i) acc[i] = mfma_bf(ldsr128(st + 8192 + (2 * (3 + i) + ks) * 1024 + 16 * lane), bfr, acc[i]);
      }
    }
  }
  const int r = lane & 15, g = lane >> 4, qq = (lane & 15) >> 2, pp = lane & 3;
  const int nd = dh == 0 ? 3 : 2;
#pragma unroll
  for (int i = 0; i < 3; ++i) {
    if (i < nd) {
      const int dt = 3 * dh + i;
      const v2u hv = trread(sm + G2_H + (16 * dt + 4 * g + qq) * 128 + 32 * tt + 8 * pp);
      const float o0 = bflo(hv.x) + rbf(acc[i][0]), o1 = bfhi(hv.x) + rbf(acc[i][1]);
      const float o2 = bflo(hv.y) + rbf(acc[i][2]), o3 = bfhi(hv.y) + rbf(acc[i][3]);
      *(GAS v2u*)(p.out + (size_t)(t0 + 16 * tt + r) * D + d0 + 16 * dt + 4 * g) = (v2u){pkbf(o0, o1), pkbf(o2, o3)};
    }
  }
}

extern "C" void kernel_launch(void* const* d_in, const int* in_sizes, int n_in, void* d_out, int out_size, void* d_ws,
                              size_t ws_size, hipStream_t stream) {
  Params p;
  p.x = (const u16*)d_in[0];
  p.logA = (const u16*)d_in[1];
  p.Aim = (const u16*)d_in[2];
  p.Cre = (const u16*)d_in[3];
  p.Cim = (const u16*)d_in[4];
  p.logdt = (const u16*)d_in[5];
  p.Dsk = (const u16*)d_in[6];
  p.w1 = (const u16*)d_in[8];
  p.w3 = (const u16*)d_in[9];
  p.w2 = (const u16*)d_in[10];
  p.out = (u16*)d_out;
  p.ws = (unsigned char*)d_ws;
  (void)hipFuncSetAttribute((const void*)kA, hipFuncAttributeMaxDynamicSharedMemorySize, A_LDS);
  (void)hipFuncSetAttribute((const void*)kG1, hipFuncAttributeMaxDynamicSharedMemorySize, G1_LDS);
  (void)hipFuncSetAttribute((const void*)kG2, hipFuncAttributeMaxDynamicSharedMemorySize, G2_LDS);
  kA<<<256, 512, A_LDS, stream>>>(p);
  kG1<<<256, 512, G1_LDS, stream>>>(p);
  kG2<<<256, 512, G2_LDS, stream>>>(p);
}
```

```cpp
#include <hip/hip_runtime.h>
#include <hip/hip_bf16.h>
#include <stdint.h>
#include <type_traits>

#define GAS __attribute__((address_space(1)))
#define LAS __attribute__((address_space(3)))
#define DI __device__ __forceinline__

typedef unsigned short u16;
typedef __bf16 bf16x8 __attribute__((ext_vector_type(8)));
typedef __bf16 bf16x2 __attribute__((ext_vector_type(2)));
typedef _Float16 f16x8 __attribute__((ext_vector_type(8)));
typedef _Float16 f16x2 __attribute__((ext_vector_type(2)));
typedef short s16x4 __attribute__((ext_vector_type(4)));
typedef float f32x4 __attribute__((ext_vector_type(4)));
typedef float f32x2 __attribute__((ext_vector_type(2)));
typedef unsigned v4u __attribute__((ext_vector_type(4)));
typedef unsigned v2u __attribute__((ext_vector_type(2)));
typedef GAS unsigned gu32;
typedef GAS unsigned long long gu64;

constexpr int L = 2048, D = 640, NM = 128, F = 1728;

constexpr size_t WS_GEN = 0;
constexpr size_t WS_SEEN = 1024;
constexpr size_t WS_STATS = 4096;
constexpr size_t WS_CS = 32768;
constexpr size_t WS_HT = 65536;
constexpr size_t WS_W13 = WS_HT + (size_t)D * L * 2;
constexpr size_t WS_W2 = WS_W13 + (size_t)16 * 10 * 28 * 1024;
constexpr size_t WS_HM = WS_W2 + (size_t)8 * 18 * 15 * 1024;
constexpr size_t WS_END = WS_HM + (size_t)128 * 54 * 1024;

struct Params {
  const u16* x;
  const u16* logA;
  const u16* Aim;
  const u16* Cre;
  const u16* Cim;
  const u16* logdt;
  const u16* Dsk;
  const u16* w1;
  const u16* w3;
  const u16* w2;
  u16* out;
  unsigned char* ws;
};

DI float bf2f(unsigned b) { return __builtin_bit_cast(float, b << 16); }
DI float bflo(unsigned v) { return __builtin_bit_cast(float, v << 16); }
DI float bfhi(unsigned v) { return __builtin_bit_cast(float, v & 0xffff0000u); }
DI unsigned pkbf(float a, float b) {
  bf16x2 r = {(__bf16)a, (__bf16)b};
  return __builtin_bit_cast(unsigned, r);
}
DI unsigned pkh(float a, float b) {
  f16x2 r = {(_Float16)a, (_Float16)b};
  return __builtin_bit_cast(unsigned, r);
}
DI float rbf(float a) { return bflo(pkbf(a, a)); }
DI float hlo(unsigned v) { return (float)__builtin_bit_cast(f16x2, v)[0]; }
DI float hhi(unsigned v) { return (float)__builtin_bit_cast(f16x2, v)[1]; }

template <int CTRL> DI float dppf(float v) {
  return __builtin_bit_cast(float, __builtin_amdgcn_update_dpp(0, __builtin_bit_cast(int, v), CTRL, 0xf, 0xf, true));
}
DI float xrow16(float v) {
  unsigned u = __builtin_bit_cast(unsigned, v);
  auto a = __builtin_amdgcn_permlane16_swap(u, u, false, false);
  v = __builtin_bit_cast(float, (unsigned)a[0]) + __builtin_bit_cast(float, (unsigned)a[1]);
  u = __builtin_bit_cast(unsigned, v);
  auto b = __builtin_amdgcn_permlane32_swap(u, u, false, false);
  return __builtin_bit_cast(float, (unsigned)b[0]) + __builtin_bit_cast(float, (unsigned)b[1]);
}
DI float wave_sum(float v) {
  v += dppf<0x128>(v);
  v += dppf<0x124>(v);
  v += dppf<0x122>(v);
  v += dppf<0x121>(v);
  return xrow16(v);
}
template <int CTRL> DI unsigned dppu(unsigned v) { return (unsigned)__builtin_amdgcn_update_dpp(0, (int)v, CTRL, 0xf, 0xf, true); }
DI float wave_max_abs(unsigned u) {
  u = max(u, dppu<0x128>(u));
  u = max(u, dppu<0x124>(u));
  u = max(u, dppu<0x122>(u));
  u = max(u, dppu<0x121>(u));
  auto a = __builtin_amdgcn_permlane16_swap(u, u, false, false);
  u = max((unsigned)a[0], (unsigned)a[1]);
  auto b = __builtin_amdgcn_permlane32_swap(u, u, false, false);
  return __builtin_bit_cast(float, max((unsigned)b[0], (unsigned)b[1]));
}
DI f32x4 mfma_bf(v4u a, v4u b, f32x4 c) {
  return __builtin_amdgcn_mfma_f32_16x16x32_bf16(__builtin_bit_cast(bf16x8, a), __builtin_bit_cast(bf16x8, b), c, 0, 0, 0);
}
DI f32x4 mfma_h(v4u a, v4u b, f32x4 c) {
  return __builtin_amdgcn_mfma_f32_16x16x32_f16(__builtin_bit_cast(f16x8, a), __builtin_bit_cast(f16x8, b), c, 0, 0, 0);
}
DI v2u trread(LAS char* p) {
  s16x4 v = __builtin_amdgcn_ds_read_tr16_b64_v4i16((LAS s16x4*)p);
  return __builtin_bit_cast(v2u, v);
}
DI v4u ldsr128(LAS char* p) { return *(LAS v4u*)p; }
DI v2u ldsr64(LAS char* p) { return *(LAS v2u*)p; }
DI void glds16(const void* g, LAS char* l) {
  __builtin_amdgcn_global_load_lds((const GAS void*)g, (LAS void*)l, 16, 0, 0);
}
#define RLX_AGENT __ATOMIC_RELAXED, __HIP_MEMORY_SCOPE_AGENT
#define WAITV(n) asm volatile("s_waitcnt vmcnt(" #n ")" ::: "memory")
#define BAR() do { asm volatile("s_waitcnt lgkmcnt(0)" ::: "memory"); __builtin_amdgcn_s_barrier(); asm volatile("" ::: "memory"); } while (0)

#define TS(k, i)
#define TC(k, i)
#define TCR(v)
#define TCW(k, i, v)
#define TCDECL
struct Stamps {};
#define SC(st, i)
#define SCW(st, k)
DI f32x2 cmul(f32x2 a, f32x2 b) {
  f32x2 t, r;
  asm("v_pk_mul_f32 %0, %1, %2 op_sel_hi:[1,0]" : "=v"(t) : "v"(a), "v"(b));
  asm("v_pk_fma_f32 %0, %1, %2, %3 op_sel:[1,1,0] op_sel_hi:[0,1,1] neg_lo:[0,1,0]" : "=v"(r) : "v"(a), "v"(b), "v"(t));
  return r;
}
DI f32x2 unbf(unsigned v) { return (f32x2){bflo(v), bfhi(v)}; }
DI f32x2 fma2(f32x2 a, f32x2 b, f32x2 c) { return __builtin_elementwise_fma(a, b, c); }

template <int I, int N, class Fn> DI void sfor(Fn&& f) {
  if constexpr (I < N) { f(std::integral_constant<int, I>{}); sfor<I + 1, N>(f); }
}
DI void a_mfma(f32x4& c, const v4u& a, const v4u& b) {
  asm volatile("v_mfma_f32_16x16x32_bf16 %0, %1, %2, %0" : "+v"(c) : "v"(a), "v"(b));
}
DI void a_mfma_h(f32x4& c, const v4u& a, const v4u& b) {
  asm volatile("v_mfma_f32_16x16x32_f16 %0, %1, %2, %0" : "+v"(c) : "v"(a), "v"(b));
}
template <int OFF> DI void a_dsr64(v2u& d, unsigned addr) {
  asm volatile("ds_read_b64 %0, %1 offset:%2" : "=v"(d) : "v"(addr), "n"(OFF));
}
template <int OFF> DI void a_dsr128(v4u& d, unsigned addr) {
  asm volatile("ds_read_b128 %0, %1 offset:%2" : "=v"(d) : "v"(addr), "n"(OFF));
}
template <int OFF> DI void a_dstr(v2u& d, unsigned addr) {
  asm volatile("ds_read_b64_tr_b16 %0, %1 offset:%2" : "=v"(d) : "v"(addr), "n"(OFF));
}
DI void a_dma(unsigned lds, unsigned voff, const void* gbase) {
  asm volatile("s_mov_b32 m0, %0\n\ts_nop 0\n\tglobal_load_lds_dwordx4 %1, %2" ::"s"(lds), "v"(voff), "s"(gbase) : "memory");
}
template <int OFF> DI void a_ds_tid(unsigned v) { asm volatile("ds_write_addtid_b32 %0 offset:%1" ::"v"(v), "n"(OFF) : "memory"); }
DI void a_m0(unsigned lds) { asm volatile("s_mov_b32 m0, %0" ::"s"(lds) : "memory"); }
DI void a_m0n(unsigned lds) { asm volatile("s_mov_b32 m0, %0\n\ts_nop 0" ::"s"(lds) : "memory"); }
template <int OFF> DI void a_dma_o(unsigned voff, const void* gbase) {
  asm volatile("global_load_lds_dwordx4 %0, %1 offset:%2" ::"v"(voff), "s"(gbase), "n"(OFF) : "memory");
}
DI void a_dot_sq(float& c, unsigned v) { asm volatile("v_dot2c_f32_bf16 %0, %1, %1" : "+v"(c) : "v"(v)); }
DI void a_dot_one(float& c, unsigned v) { asm volatile("v_dot2c_f32_bf16 %0, 0x3f803f80, %1" : "+v"(c) : "v"(v)); }
template <int HI> DI void a_stat(float& sum, float& sq, unsigned v) {
  float x;
  if constexpr (HI)
    asm volatile("v_and_b32 %2, 0xffff0000, %3\n\tv_add_f32 %0, %0, %2\n\tv_fmac_f32 %1, %2, %2" : "+v"(sum), "+v"(sq), "=&v"(x) : "v"(v));
  else
    asm volatile("v_lshlrev_b32 %2, 16, %3\n\tv_add_f32 %0, %0, %2\n\tv_fmac_f32 %1, %2, %2" : "+v"(sum), "+v"(sq), "=&v"(x) : "v"(v));
}
template <int N> DI void a_waitl() { static_assert(N >= 0 && N <= 15, "lgkmcnt"); asm volatile("s_waitcnt lgkmcnt(%0)" ::"n"(N) : "memory"); }
template <int N> DI void a_waitv() { asm volatile("s_waitcnt vmcnt(%0)" ::"n"(N) : "memory"); }
template <class T> DI void a_pin(T& v) { asm volatile("" : "+v"(v)); }
DI void a_lgkm0() { asm volatile("s_waitcnt lgkmcnt(0)" ::: "memory"); }
DI void a_bar() { asm volatile("s_barrier" ::: "memory"); }
DI unsigned lds_addr(LAS char* p) { return (unsigned)(size_t)p; }

constexpr int T_CH = 1344;
constexpr int A_T = 0;
constexpr int A_SC = 4 * T_CH;
constexpr int KG_ROW = 272;
constexpr int KG_GA = 21 * KG_ROW, KG_EB = 32 * KG_ROW;
constexpr int A_KG = A_SC + 64;
constexpr int A_GA = A_KG, A_EB = A_GA + 8 * KG_GA;
static_assert(7 * KG_EB < 65536 && A_EB + 31 * KG_ROW < 65536, "ds_write_addtid: M0 and offset are 16 bits each");
constexpr int KC_COPY = 4416, KC_CH = 4 * KC_COPY;
constexpr int A_EX = A_KG + 4 * KC_CH;
constexpr int A_U = A_EB + 8 * KG_EB;
constexpr int U_CH = 4608;
constexpr int A_X = A_U + 4 * U_CH;
constexpr int A_PX = A_X;
constexpr int A_LDS = A_X + 4 * 4096;
static_assert(A_EX + 8 * 4096 <= A_U, "lds map");
static_assert(A_LDS <= 163840 && A_KG % 64 == 0, "lds");

struct ExpTab {
  unsigned v[256 * 12];
  constexpr ExpTab() : v() {
    for (int c = 0; c < 256; ++c)
      for (int i = 0; i < 12; ++i) {
        const int l = 2051 - 8 * c - (i < 11 ? i : 10);
        int sh = 0;
        while ((l >> sh) >= 256) ++sh;
        int idx = l;
        if (sh > 0) idx = 128 * sh + ((l + (1 << (sh - 1)) - 1 + ((l >> sh) & 1)) >> sh);
        v[c * 12 + i] = 2u * (unsigned)idx;
      }
  }
};
__device__ const ExpTab g_exp_tab = ExpTab();

template <int OFF> DI void g_ld32(unsigned& d, unsigned voff, const void* base) {
  asm volatile("global_load_dword %0, %1, %2 offset:%3" : "=v"(d) : "v"(voff), "s"(base), "n"(OFF) : "memory");
}
DI void g_ld32_sc1(unsigned& d, unsigned voff, const void* base) {
  asm volatile("global_load_dword %0, %1, %2 sc1" : "=v"(d) : "v"(voff), "s"(base) : "memory");
}
DI void g_ld16(unsigned& d, unsigned voff, const void* base) {
  asm volatile("global_load_ushort %0, %1, %2" : "=v"(d) : "v"(voff), "s"(base) : "memory");
}
template <int OFF> DI void g_ld128(v4u& d, unsigned voff, const void* base) {
  asm volatile("global_load_dwordx4 %0, %1, %2 offset:%3" : "=v"(d) : "v"(voff), "s"(base), "n"(OFF) : "memory");
}
template <int OFF> DI void g_ld128_sc1(v4u& d, unsigned voff, const void* base) {
  asm volatile("global_load_dwordx4 %0, %1, %2 offset:%3 sc1" : "=v"(d) : "v"(voff), "s"(base), "n"(OFF) : "memory");
}
template <int OFF> DI void g_ld64(v2u& d, unsigned voff, const void* base) {
  asm volatile("global_load_dwordx2 %0, %1, %2 offset:%3" : "=v"(d) : "v"(voff), "s"(base), "n"(OFF) : "memory");
}

DI void ln_rows(const Params& p, int row0, int lane) {
  const unsigned gen = __hip_atomic_load((gu32*)(p.ws + WS_GEN), RLX_AGENT);
  const int row = row0 + (lane >> 4);
  v4u xr[5];
#pragma unroll
  for (int i = 0; i < 5; ++i) xr[i] = *(const GAS v4u*)(p.x + (size_t)row * D + 8 * ((lane & 15) + 16 * i));
  f32x2 s2[2] = {{0, 0}, {0, 0}}, q2[2] = {{0, 0}, {0, 0}};
#pragma unroll
  for (int i = 0; i < 5; ++i)
#pragma unroll
    for (int e = 0; e < 4; ++e) {
      const unsigned d = xr[i][e];
      const f32x2 v = unbf(d);
      s2[e & 1] += v;
      q2[e & 1] = fma2(v, v, q2[e & 1]);
    }
  float sm_ = (s2[0].x + s2[0].y) + (s2[1].x + s2[1].y), sq = (q2[0].x + q2[0].y) + (q2[1].x + q2[1].y);
  sm_ += dppf<0x128>(sm_); sq += dppf<0x128>(sq);
  sm_ += dppf<0x124>(sm_); sq += dppf<0x124>(sq);
  sm_ += dppf<0x122>(sm_); sq += dppf<0x122>(sq);
  sm_ += dppf<0x121>(sm_); sq += dppf<0x121>(sq);
  const float mean = sm_ * (1.0f / D);
  const float var = fmaxf(sq * (1.0f / D) - mean * mean, 0.f);
  const float sd = rbf(__builtin_amdgcn_sqrtf(rbf(rbf(var) + 1e-5f)));
  if ((lane & 15) == 0)
    __hip_atomic_store((gu64*)(p.ws + WS_STATS) + row, ((unsigned long long)(gen + 1u) << 32) | pkbf(mean, sd), RLX_AGENT);
}

template <int HALF>
DI void conv_tail(const Params& p, LAS char* sm, f32x4 (&acc)[8], int lane, int w, int ch, int c0, unsigned vdsk, Stamps& stp) {
  const int r = lane & 15, q = lane >> 4;
  LAS char* mine = sm + A_EX + w * 4096 + 16 * lane;
  LAS char* other = sm + A_EX + (w ^ 4) * 4096 + 16 * lane;
#pragma unroll
  for (int i = 0; i < 4; ++i) *(LAS f32x4*)(mine + 1024 * i) = acc[4 * (1 - HALF) + i];
  LAS char* up = sm + A_U + ch * U_CH + 512 + 2048 * HALF + 32 * r + 8 * q;
  LAS char* xp = sm + A_X + ch * 4096 + 2048 * HALF + 32 * r + 8 * q;
  v2u uv[4], xv[4];
#pragma unroll
  for (int i = 0; i < 4; ++i) { uv[i] = ldsr64(up + 512 * i); xv[i] = ldsr64(xp + 512 * i); }
  const float scale = *(LAS float*)(sm + A_SC + 4 * ch);
  const float dsk = bf2f(vdsk);
  f32x2 base[4][2];
#pragma unroll
  for (int i = 0; i < 4; ++i) {
    base[i][0] = (f32x2){__builtin_fmaf(hlo(uv[i].x), dsk, bflo(xv[i].x)), __builtin_fmaf(hhi(uv[i].x), dsk, bfhi(xv[i].x))};
    base[i][1] = (f32x2){__builtin_fmaf(hlo(uv[i].y), dsk, bflo(xv[i].y)), __builtin_fmaf(hhi(uv[i].y), dsk, bfhi(xv[i].y))};
  }
  BAR();
  const f32x2 sc2 = {scale, scale};
  v2u ho[4];
#pragma unroll
  for (int i = 0; i < 4; ++i) {
    const f32x4 y = acc[4 * HALF + i] + *(LAS f32x4*)(other + 1024 * i);
    const f32x2 h0 = fma2((f32x2){y[0], y[1]}, sc2, base[i][0]), h1 = fma2((f32x2){y[2], y[3]}, sc2, base[i][1]);
    ho[i] = (v2u){pkbf(h0.x, h0.y), pkbf(h1.x, h1.y)};
  }
  SC(stp, 8);
  u16* hT = (u16*)(p.ws + WS_HT) + (size_t)(c0 + ch) * L + 1024 * HALF + 256 * (q & 1) + 16 * r + 8 * (q >> 1);
#pragma unroll
  for (int i = 0; i < 4; i += 2) {
    auto sx = __builtin_amdgcn_permlane16_swap(ho[i].x, ho[i + 1].x, false, false);
    auto sy = __builtin_amdgcn_permlane16_swap(ho[i].y, ho[i + 1].y, false, false);
    *(GAS v4u*)(hT + 256 * i) = (v4u){(unsigned)sx[0], (unsigned)sy[0], (unsigned)sx[1], (unsigned)sy[1]};
  }
}

struct ConvSet { v2u al[8], ah[8]; v4u b[8]; };

DI void conv_role(const Params& p, LAS char* sm, int tid, int lane, int w, int cgp) {
  Stamps stp;
  SC(stp, 0);
  const int c0 = 4 * cgp;
  const unsigned smb = lds_addr(sm);
  const int kch = c0 + (w >> 1), hf = w & 1, n = 64 * hf + lane;
  const int ec = tid & 255, epair = tid >> 8;
  unsigned gen, vdt, vla, vai, vcr, vci, vcr2, vci2, vdsk;
  v4u et[3];
  v2u xg[4];
  const unsigned go = (unsigned)(4 * tid) * (D * 2);
  const u16* xc = p.x + c0;
  {
    const unsigned zero = 0, po = 2 * (kch * NM + n), po2 = 2 * (kch * NM + (n ^ 64));
    g_ld16(vdt, zero, p.logdt + kch);
    g_ld16(vla, po, p.logA); g_ld16(vai, po, p.Aim); g_ld16(vcr, po, p.Cre); g_ld16(vci, po, p.Cim);
    g_ld16(vcr2, po2, p.Cre); g_ld16(vci2, po2, p.Cim);
    g_ld32_sc1(gen, zero, p.ws + WS_GEN);
    g_ld16(vdsk, zero, p.Dsk + c0 + (w & 3));
    g_ld128<0>(et[0], 48 * ec, g_exp_tab.v); g_ld128<16>(et[1], 48 * ec, g_exp_tab.v); g_ld128<32>(et[2], 48 * ec, g_exp_tab.v);
    asm volatile("s_barrier" ::: "memory");
    g_ld64<0>(xg[0], go, xc); g_ld64<1280>(xg[1], go, xc);
  }
  SC(stp, 10);
  asm volatile("s_waitcnt vmcnt(2)"
               : "+v"(gen), "+v"(vdt), "+v"(vla), "+v"(vai), "+v"(vcr), "+v"(vci), "+v"(vcr2), "+v"(vci2), "+v"(vdsk), "+v"(et[0]),
                 "+v"(et[1]), "+v"(et[2])::"memory");
  const unsigned tag = gen + 1u;
  SC(stp, 1);
  float kscale;
  {
    const float dt = rbf(__builtin_amdgcn_exp2f(bf2f(vdt) * 1.4426950408889634f));
    const float are = -rbf(__builtin_amdgcn_exp2f(bf2f(vla) * 1.4426950408889634f)) * dt, aim = bf2f(vai) * dt;
    const float cmax = wave_max_abs(max(max((vcr & 0x7fffu) << 16, (vci & 0x7fffu) << 16), max((vcr2 & 0x7fffu) << 16, (vci2 & 0x7fffu) << 16)));
    const float cinv = cmax > 0.f ? __builtin_amdgcn_rcpf(cmax) : 0.f;
    kscale = 2.0f * dt * cmax;
    const float al = are * 1.4426950408889634f, ar = aim * 0.15915494309189535f;
    auto cpow = [&](float pw) -> f32x2 {
      const float e = __builtin_amdgcn_exp2f(al * pw);
      const float f = __builtin_amdgcn_fractf(ar * pw);
      return (f32x2){e * __builtin_amdgcn_cosf(f), e * __builtin_amdgcn_sinf(f)};
    };
    const f32x2 w1 = cpow(1.f), w8 = cpow(8.f), w16 = cpow(16.f), w32 = cpow(32.f), W1 = cpow(256.f);
    f32x2 e[4] = {{1.f, 0.f}, w8, w16, cmul(w8, w16)};
    a_m0n(smb + w * KG_EB);
    sfor<0, 8>([&](auto J) {
      constexpr int j = 4 * decltype(J)::value;
      unsigned pk[4];
#pragma unroll
      for (int i = 0; i < 4; ++i) pk[i] = pkh(e[i].x, e[i].y);
      a_ds_tid<A_EB + (j + 0) * KG_ROW>(pk[0]); a_ds_tid<A_EB + (j + 1) * KG_ROW>(pk[1]);
      a_ds_tid<A_EB + (j + 2) * KG_ROW>(pk[2]); a_ds_tid<A_EB + (j + 3) * KG_ROW>(pk[3]);
      if constexpr (j + 4 < 32) {
#pragma unroll
        for (int i = 0; i < 4; ++i) e[i] = cmul(e[i], w32);
      }
    });
    g_ld64<2560>(xg[2], go, xc);
    const f32x2 v1 = {w1.x, -w1.y}, V1 = {W1.x, -W1.y};
    const f32x2 v2 = cmul(v1, v1), v4 = cmul(v2, v2);
    f32x2 g[21];
    g[0] = (f32x2){bf2f(vcr) * cinv, -bf2f(vci) * cinv};
    g[1] = cmul(g[0], v1); g[2] = cmul(g[0], v2); g[3] = cmul(g[1], v2);
#pragma unroll
    for (int r = 4; r < 8; ++r) g[r] = cmul(g[r - 4], v4);
    const f32x2 V2 = cmul(V1, V1), V3 = cmul(V2, V1), V4 = cmul(V2, V2);
#pragma unroll
    for (int bb = 0; bb < 4; ++bb) g[8 + bb] = cmul(g[2 * bb], V1);
    g[12] = cmul(g[0], V2); g[13] = cmul(g[4], V2); g[14] = cmul(g[0], V3); g[15] = cmul(g[4], V3);
    g[16] = cmul(g[0], V4);
    g[17] = cmul(g[16], V1); g[18] = cmul(g[16], V2); g[19] = cmul(g[16], V3); g[20] = cmul(g[16], V4);
    a_m0n(smb + w * KG_GA);
    sfor<0, 21>([&](auto R) { constexpr int rw = R; a_ds_tid<A_GA + rw * KG_ROW>(pkh(g[rw].x, g[rw].y)); });
    g_ld64<3840>(xg[3], go, xc);
  }
  f32x4 d0, d1;
  {
    const int r = lane & 15, q = lane >> 4;
    f32x4 d[2][2];
#pragma unroll
    for (int i = 0; i < 4; ++i) { d[i >> 1][i & 1] = (f32x4){0, 0, 0, 0}; a_pin(d[i >> 1][i & 1]); }
    a_lgkm0();
    const unsigned ga = smb + A_GA + w * KG_GA + r * KG_ROW + 16 * q, eb = smb + A_EB + w * KG_EB + r * KG_ROW + 16 * q;
    v4u a[2][4], bb[2][4];
    sfor<0, 4>([&](auto S) {
      constexpr int s = S;
      a_dsr128<64 * s>(a[0][s], ga); a_dsr128<64 * s + 16 * KG_ROW>(a[1][s], ga);
      a_dsr128<64 * s>(bb[0][s], eb); a_dsr128<64 * s + 16 * KG_ROW>(bb[1][s], eb);
    });
    a_lgkm0();
#pragma unroll
    for (int s = 0; s < 4; ++s)
#pragma unroll
      for (int i = 0; i < 4; ++i) a_mfma_h(d[i >> 1][i & 1], a[i >> 1][s], bb[i & 1][s]);
    asm volatile("s_nop 15" ::: "memory");
#pragma unroll
    for (int i = 0; i < 4; ++i) a_pin(d[i >> 1][i & 1]);
    LAS char* px = sm + A_PX + w * 2048 + 16 * lane;
    if (tid == 128 * (w >> 1)) *(LAS float*)(sm + A_SC + 4 * (w >> 1)) = kscale;
    if (hf == 0) {
      *(LAS f32x4*)px = d[0][1]; *(LAS f32x4*)(px + 1024) = d[1][1]; d0 = d[0][0]; d1 = d[1][0];
    } else {
      *(LAS f32x4*)px = d[0][0]; *(LAS f32x4*)(px + 1024) = d[1][0]; d0 = d[0][1]; d1 = d[1][1];
    }
  }
  SC(stp, 2);
  BAR();
  if (tid == 0) __hip_atomic_store((gu32*)(p.ws + WS_SEEN) + cgp, tag, RLX_AGENT);
  {
    const int r = lane & 15, q = lane >> 4;
    LAS char* po = sm + A_PX + (w ^ 1) * 2048 + 16 * lane;
    d0 += *(LAS f32x4*)po;
    d1 += *(LAS f32x4*)(po + 1024);
    const int j = 16 * hf + r;
    LAS char* T = sm + A_T + (w >> 1) * T_CH;
    const int ib = q < 2 ? 4 * q + 8 * j : (q == 2 ? 256 + 4 * j : 384 + 2 * j);
    const int is2 = q == 3 ? 64 : 2;
    *(LAS unsigned*)(T + 2 * ib) = pkh(d0[0], d0[1]);
    *(LAS unsigned*)(T + 2 * (ib + is2)) = pkh(d0[2], d0[3]);
#pragma unroll
    for (int i = 0; i < 4; ++i) {
      const int i1 = q == 0 ? 512 + 32 * i + j : ((q == 1 && i == 0 && j == 0) ? 640 : 641 + i);
      *(LAS _Float16*)(T + 2 * i1) = (_Float16)d1[i];
    }
  }
  SC(stp, 3);
  BAR();
  v4u sg[2];
  g_ld128_sc1<0>(sg[0], 32 * tid, p.ws + WS_STATS);
  g_ld128_sc1<16>(sg[1], 32 * tid, p.ws + WS_STATS);
  {
    const unsigned eo[11] = {et[0].x, et[0].y, et[0].z, et[0].w, et[1].x, et[1].y, et[1].z, et[1].w, et[2].x, et[2].y, et[2].z};
    typedef unsigned short u16x2 __attribute__((ext_vector_type(2)));
#pragma unroll
    for (int cc = 0; cc < 2; ++cc) {
      const int ch = 2 * epair + cc;
      LAS char* T = sm + A_T + ch * T_CH;
      unsigned d[6];
#pragma unroll
      for (int i = 0; i < 5; ++i) {
        const u16x2 pr = {*(LAS u16*)(T + eo[2 * i]), *(LAS u16*)(T + eo[2 * i + 1])};
        d[i] = __builtin_bit_cast(unsigned, pr);
      }
      d[5] = *(LAS u16*)(T + eo[10]);
      LAS char* K = sm + A_KG + ch * KC_CH + 16 * ec;
      v4u c3 = {d[0], d[1], d[2], d[3]};
      v4u c1 = {d[1], d[2], d[3], d[4]};
      v4u c2, c0v;
#pragma unroll
      for (int i = 0; i < 4; ++i) {
        c2[i] = __builtin_amdgcn_alignbit(d[i + 1], d[i], 16);
        c0v[i] = __builtin_amdgcn_alignbit(d[i + 2], d[i + 1], 16);
      }
      *(LAS v4u*)(K + 0 * KC_COPY) = c0v;
      *(LAS v4u*)(K + 1 * KC_COPY) = c1;
      *(LAS v4u*)(K + 2 * KC_COPY) = c2;
      *(LAS v4u*)(K + 3 * KC_COPY) = c3;
    }
    if (tid < 256) {
      const int ch = tid >> 6, pp = (tid >> 4) & 3, i = tid & 15;
      const int l = pp - i;
      const u16 val = l >= 0 ? *(LAS u16*)(sm + A_T + ch * T_CH + 2 * l) : (u16)0;
      *(LAS u16*)(sm + A_KG + ch * KC_CH + pp * KC_COPY + 2 * (2048 + i)) = val;
    }
    if (tid >= 384) {
      v4u z = {0, 0, 0, 0};
      *(LAS v4u*)(sm + A_U + ((tid - 384) >> 5) * U_CH + 16 * (tid & 31)) = z;
    }
  }
  SC(stp, 4);
  {
    asm volatile("s_waitcnt vmcnt(0)" : "+v"(sg[0]), "+v"(sg[1]), "+v"(xg[0]), "+v"(xg[1]), "+v"(xg[2]), "+v"(xg[3])::"memory");
    for (unsigned spin = 0; spin < (1u << 20); ++spin) {
      if (__all(sg[0].y == tag && sg[0].w == tag && sg[1].y == tag && sg[1].w == tag)) break;
      __builtin_amdgcn_s_sleep(1);
      g_ld128_sc1<0>(sg[0], 32 * tid, p.ws + WS_STATS);
      g_ld128_sc1<16>(sg[1], 32 * tid, p.ws + WS_STATS);
      asm volatile("s_waitcnt vmcnt(0)" : "+v"(sg[0]), "+v"(sg[1])::"memory");
    }
    SC(stp, 5);
    f32x2 ua[4], ub[4];
#pragma unroll
    for (int k = 0; k < 4; ++k) {
      const unsigned st = k == 0 ? sg[0].x : k == 1 ? sg[0].z : k == 2 ? sg[1].x : sg[1].z;
      const float rs = __builtin_amdgcn_rcpf(bfhi(st)), mr = -bflo(st) * rs;
      ua[k] = fma2(unbf(xg[k].x), (f32x2){rs, rs}, (f32x2){mr, mr});
      ub[k] = fma2(unbf(xg[k].y), (f32x2){rs, rs}, (f32x2){mr, mr});
    }
    LAS char* ud = sm + A_U + 512 + 8 * tid;
    *(LAS v2u*)(ud + 0 * U_CH) = (v2u){pkh(ua[0].x, ua[1].x), pkh(ua[2].x, ua[3].x)};
    *(LAS v2u*)(ud + 1 * U_CH) = (v2u){pkh(ua[0].y, ua[1].y), pkh(ua[2].y, ua[3].y)};
    *(LAS v2u*)(ud + 2 * U_CH) = (v2u){pkh(ub[0].x, ub[1].x), pkh(ub[2].x, ub[3].x)};
    *(LAS v2u*)(ud + 3 * U_CH) = (v2u){pkh(ub[0].y, ub[1].y), pkh(ub[2].y, ub[3].y)};
    const unsigned s0 = 0x05040100u, s1 = 0x07060302u;
    LAS char* xd = sm + A_X + 8 * tid;
    *(LAS v2u*)(xd + 0 * 4096) = (v2u){__builtin_amdgcn_perm(xg[1].x, xg[0].x, s0), __builtin_amdgcn_perm(xg[3].x, xg[2].x, s0)};
    *(LAS v2u*)(xd + 1 * 4096) = (v2u){__builtin_amdgcn_perm(xg[1].x, xg[0].x, s1), __builtin_amdgcn_perm(xg[3].x, xg[2].x, s1)};
    *(LAS v2u*)(xd + 2 * 4096) = (v2u){__builtin_amdgcn_perm(xg[1].y, xg[0].y, s0), __builtin_amdgcn_perm(xg[3].y, xg[2].y, s0)};
    *(LAS v2u*)(xd + 3 * 4096) = (v2u){__builtin_amdgcn_perm(xg[1].y, xg[0].y, s1), __builtin_amdgcn_perm(xg[3].y, xg[2].y, s1)};
  }
  const int ch = w & 3, half = w >> 2, r = lane & 15, q = lane >> 4;
  f32x4 acc[8];
#pragma unroll
  for (int i = 0; i < 8; ++i) { acc[i] = (f32x4){0, 0, 0, 0}; a_pin(acc[i]); }
  const unsigned aA = smb + A_KG + ch * KC_CH + (r & 3) * KC_COPY + 2 * (2048 - 16 - 4 * (r >> 2) + 8 * q) - 512 * 7 - 64 * (4 * half + 3);
  const unsigned aB = smb + A_U + ch * U_CH + 512 + 32 * r - 32 + 16 * q - 64 * (4 * half + 3);
  SC(stp, 6);
  BAR();
  {
    ConvSet R0, R1;
    auto rd = [&](auto J, auto DD, ConvSet& R) {
      constexpr int j = J, dd = DD;
      if constexpr (j >= 2 && j < 10) a_dsr128<512 * (j - 2) + 64 * (3 - dd)>(R.b[j - 2], aB);
      else {
        constexpr int jj = j < 2 ? j : j - 8, g = jj >> 1;
        if constexpr (jj & 1) a_dsr64<512 * (7 - g) + 64 * (3 - dd) + 8>(R.ah[g], aA);
        else a_dsr64<512 * (7 - g) + 64 * (3 - dd)>(R.al[g], aA);
      }
    };
    auto block = [&](auto DD, ConvSet& X, ConvSet& Y) {
      constexpr int dd = DD;
      sfor<0, 8>([&](auto G) {
        constexpr int g = G, m0 = g * 8 - g * (g - 1) / 2;
        const v4u a = {X.al[g].x, X.al[g].y, X.ah[g].x, X.ah[g].y};
        sfor<g, 8>([&](auto N) {
          constexpr int nn = N, m = m0 + nn - g;
          if constexpr (dd == 0) {
            constexpr int need = g == 0 ? 2 + nn : 9 + 2 * g, mine = m < 24 ? m : 24, out = 23 - need + mine;
            if constexpr (g == 0 ? nn == 7 : nn == g) a_waitl<(out < 15 ? out : 15)>();
          }
          a_mfma_h(acc[nn], a, X.b[nn - g]);
          if constexpr (dd < 3 && m < 24) rd(std::integral_constant<int, m>{}, std::integral_constant<int, dd + 1>{}, Y);
        });
      });
      if constexpr (dd < 3) a_lgkm0();
    };
    sfor<0, 24>([&](auto J) { rd(J, std::integral_constant<int, 0>{}, R0); });
    a_waitl<15>();
    block(std::integral_constant<int, 0>{}, R0, R1);
    block(std::integral_constant<int, 1>{}, R1, R0);
    block(std::integral_constant<int, 2>{}, R0, R1);
    block(std::integral_constant<int, 3>{}, R1, R0);
    asm volatile("s_nop 15\n\ts_nop 3" ::: "memory");
#pragma unroll
    for (int i = 0; i < 8; ++i) a_pin(acc[i]);
  }
  SC(stp, 7);
  if (half == 0) conv_tail<0>(p, sm, acc, lane, w, ch, c0, vdsk, stp);
  else conv_tail<1>(p, sm, acc, lane, w, ch, c0, vdsk, stp);
  if (cgp == 0 && w == 0) {
    const gu32* seen = (const gu32*)(p.ws + WS_SEEN);
    for (unsigned spin = 0; spin < (1u << 20); ++spin) {
      bool ok = true;
#pragma unroll
      for (int k = 0; k < 3; ++k) {
        const int i = lane + 64 * k;
        ok &= i >= 160 || __hip_atomic_load(seen + (i < 160 ? i : 0), RLX_AGENT) == tag;
      }
      if (__all(ok)) break;
      __builtin_amdgcn_s_sleep(1);
    }
    if (lane == 0) __hip_atomic_store((gu32*)(p.ws + WS_GEN), tag, RLX_AGENT);
  }
  SC(stp, 9);
  SCW(stp, 0);
}

DI void helper_role(const Params& p, LAS char* sm, int lane, int w, int hidx) {
  const int g = lane >> 4, qq = (lane & 15) >> 2, pp = lane & 3;
  if (hidx < 54) {
    const int mat = hidx / 27, cb = hidx % 27;
    const u16* W = mat ? p.w3 : p.w1;
    LAS char* buf = sm + w * 12288;
#pragma unroll
    for (int i = 0; i < 3; ++i) {
      const int s = w + 8 * i;
      if (s < 20) {
#pragma unroll
        for (int k = 0; k < 4; ++k)
          glds16(W + (size_t)(32 * s + 8 * k + (lane >> 3)) * F + 64 * cb + 8 * (lane & 7), buf + i * 4096 + k * 1024);
      }
    }
    WAITV(0);
    float cs[4] = {0, 0, 0, 0};
#pragma unroll
    for (int i = 0; i < 3; ++i) {
      const int s = w + 8 * i;
      if (s < 20) {
#pragma unroll
        for (int u = 0; u < 4; ++u) {
          LAS char* a = buf + i * 4096 + (8 * g + qq) * 128 + u * 32 + pp * 8;
          const v2u lo = trread(a), hi = trread(a + 4 * 128);
          const v4u fr = {lo.x, lo.y, hi.x, hi.y};
          const int un = 4 * cb + u, cgu = un < 84 ? un / 7 : 12 + (un - 84) / 6, ul = un < 84 ? un % 7 : (un - 84) % 6;
          *(GAS v4u*)(p.ws + WS_W13 + ((size_t)(cgu * 10 + (s >> 1)) * 28 + 4 * ul + 2 * mat + (s & 1)) * 1024 + 16 * lane) = fr;
          cs[u] += (bflo(fr.x) + bfhi(fr.x)) + (bflo(fr.y) + bfhi(fr.y)) + (bflo(fr.z) + bfhi(fr.z)) + (bflo(fr.w) + bfhi(fr.w));
        }
      }
    }
    LAS float* red = (LAS float*)(sm + 98304);
#pragma unroll
    for (int u = 0; u < 4; ++u) {
      const float t = xrow16(cs[u]);
      if (lane < 16) red[w * 64 + u * 16 + lane] = t;
    }
    __syncthreads();
    if (w == 0) {
      float t = 0;
#pragma unroll
      for (int i = 0; i < 8; ++i) t += red[i * 64 + lane];
      ((float*)(p.ws + WS_CS))[mat * F + 64 * cb + lane] = t;
    }
  } else {
    LAS char* buf = sm + w * 8192;
#pragma unroll
    for (int i = 0; i < 2; ++i) {
      const int blk = (hidx - 54) * 8 + w + 336 * i;
      if (blk < 540) {
        const int s = blk / 10, cb = blk % 10;
#pragma unroll
        for (int k = 0; k < 4; ++k)
          glds16(p.w2 + (size_t)(32 * s + 8 * k + (lane >> 3)) * D + 64 * cb + 8 * (lane & 7), buf + i * 4096 + k * 1024);
      }
    }
    WAITV(0);
#pragma unroll
    for (int i = 0; i < 2; ++i) {
      const int blk = (hidx - 54) * 8 + w + 336 * i;
      if (blk < 540) {
        const int s = blk / 10, cb = blk % 10;
#pragma unroll
        for (int u = 0; u < 4; ++u) {
          LAS char* a = buf + i * 4096 + (8 * g + qq) * 128 + u * 32 + pp * 8;
          const v2u lo = trread(a), hi = trread(a + 4 * 128);
          const v4u fr = {lo.x, lo.y, hi.x, hi.y};
          const int dt = 4 * cb + u;
          *(GAS v4u*)(p.ws + WS_W2 + ((size_t)((dt / 5) * 18 + s / 3) * 15 + (dt % 5) * 3 + s % 3) * 1024 + 16 * lane) = fr;
        }
      }
    }
  }
}

__global__ __launch_bounds__(512, 2) void kA(Params p) {
  extern __shared__ __attribute__((aligned(16))) char smem[];
  LAS char* sm = (LAS char*)smem;
  const int tid = threadIdx.x, lane = tid & 63, w = __builtin_amdgcn_readfirstlane(tid >> 6);
  const int b = blockIdx.x, xl = b & 7, j = b >> 3;
  TS(0, 0);
  if (j < 20) {
    conv_role(p, sm, tid, lane, w, 20 * xl + j);
  } else {
    const int hidx = (j - 20) * 8 + xl;
    if (hidx < 64) ln_rows(p, 4 * (8 * hidx + w), lane);
    helper_role(p, sm, lane, w, hidx);
  }
  TS(0, 1);
}

#define X_BLK1 1
constexpr int G1_STAGE = 16384 + 28 * 1024;
constexpr int G1_RED = 3 * G1_STAGE;
constexpr int G1_CS = G1_RED + 1024;
constexpr int G1_LDS = G1_CS + 1024;

template <int UH> struct G1Set {
  v4u a[UH ? 6 : 8];
  v2u b[2][2];
};

template <int UH>
DI void g1_wave(const Params& p, LAS char* sm, int lane, int tw, int rg, int cg, int u0, int nu, int kt0) {
  constexpr int NU = UH ? 3 : 4, UB = UH ? 4 : 0, NA = 2 * NU, NR = NA + 4, NM = 2 * NA, ND = UH ? 7 : 4;
  TCDECL;
  TCR(tc0_);
  const unsigned smb = lds_addr(sm);
  const int g = lane >> 4, qq = (lane & 15) >> 2, pp = lane & 3, r = lane & 15;
  unsigned voff[UH ? 1 : 4];
  const unsigned char* gb;
  constexpr unsigned KSTR = UH ? 28 * 1024 : 64 * L * 2;
  unsigned m0s;
  if constexpr (UH == 0) {
#pragma unroll
    for (int i = 0; i < 4; ++i) {
      const int row = 16 * tw + 4 * i + (lane >> 4);
      const int ch = (lane & 15) ^ (((row & 3) << 2) | ((row >> 2) & 3));
      voff[i] = (unsigned)(row * L * 2 + ch * 16 - 1024 * i);
    }
    gb = p.ws + WS_HT + 256 * rg;
    m0s = smb + 4096 * tw;
  } else {
    voff[0] = 16 * lane;
    gb = p.ws + WS_W13 + ((size_t)cg * 280 + 7 * tw + 3) * 1024;
    m0s = smb + 16384 + (7 * tw + 3) * 1024;
  }
  auto dma = [&](auto I, const unsigned char* base) {
    constexpr int i = I;
    if constexpr (UH == 0) a_dma_o<1024 * i>(voff[i], base);
    else a_dma_o<1024 * (i - 3)>(voff[0], base);
  };
  auto kbase = [&](int it) {
    const int blk = it / X_BLK1;
    int k = it - blk * X_BLK1 + kt0; k = k >= X_BLK1 ? k - X_BLK1 : k;
    return gb + (size_t)((unsigned)(blk * X_BLK1 + k) * KSTR);
  };
  const unsigned aA = smb + 16384 + 16 * lane;
  unsigned aB[2][2];
#pragma unroll
  for (int t = 0; t < 2; ++t)
#pragma unroll
    for (int tt = 0; tt < 2; ++tt) {
      const int row = 8 * g + 4 * tt + qq;
      const int ch = (2 * (2 * tw + t) + (pp >> 1)) ^ (((row & 3) << 2) | ((row >> 2) & 3));
      aB[t][tt] = smb + row * 256 + 16 * ch + 8 * (pp & 1);
    }
  f32x4 acc[NA][2];
#pragma unroll
  for (int i = 0; i < NA; ++i) {
    acc[i][0] = (f32x4){0, 0, 0, 0}; acc[i][1] = (f32x4){0, 0, 0, 0};
    a_pin(acc[i][0]); a_pin(acc[i][1]);
  }
  float ssum = 0, ssq = 0;
  a_pin(ssum); a_pin(ssq);
  G1Set<UH> R0, R1;

  auto rd = [&](auto J, auto KS, G1Set<UH>& R, unsigned cA, const unsigned (&cB)[2][2]) {
    constexpr int j = J, ks = KS;
    if constexpr (j < 4) a_dstr<ks * 8192>(R.b[j >> 1][j & 1], cB[j >> 1][j & 1]);
    else { constexpr int a = j - 4; a_dsr128<(4 * (UB + (a >> 1)) + 2 * (a & 1) + ks) * 1024>(R.a[a], cA); }
  };
  auto mm = [&](auto M, G1Set<UH>& R) {
    constexpr int m = M, a = m >> 1, t = m & 1;
    const v4u bb = {R.b[t][0].x, R.b[t][0].y, R.b[t][1].x, R.b[t][1].y};
    a_mfma(acc[a][t], R.a[a], bb);
  };
  auto stat = [&](auto S, G1Set<UH>& R) {
    constexpr int s = S, e = s >> 1;
    const unsigned hw = e == 0 ? R.b[UH][0].x : e == 1 ? R.b[UH][0].y : e == 2 ? R.b[UH][1].x : R.b[UH][1].y;
    a_stat<s & 1>(ssum, ssq, hw);
  };
  auto block = [&](auto KS, auto RGc, auto CNT, auto DO_DMA, G1Set<UH>& X, G1Set<UH>& Y, unsigned cA, const unsigned (&cB)[2][2],
                   const unsigned char* base, unsigned m0v) {
    constexpr int RG = RGc;
    if constexpr (DO_DMA) a_m0(m0v);
    sfor<0, NM>([&](auto M) {
      constexpr int m = M;
      constexpr int issued = RG == 0 ? 0 : (m * NR + RG - 1) / RG < NR ? (m * NR + RG - 1) / RG : NR;
      if constexpr (CNT && (m & 1) == 0) a_waitl<(NR - 1 - (4 + m / 2)) + issued>();
      mm(M, X);
      if constexpr (RG > 0) {
        constexpr int nxt = ((m + 1) * NR + RG - 1) / RG < NR ? ((m + 1) * NR + RG - 1) / RG : NR;
        sfor<issued, nxt>([&](auto J) { rd(J, KS, Y, cA, cB); });
      }
      if constexpr (DO_DMA) {
        sfor<0, ND>([&](auto Dd) { if constexpr ((decltype(Dd)::value * NM + NM / 2) / ND == m) dma(Dd, base); });
      }
      sfor<0, 8>([&](auto S) { if constexpr ((decltype(S)::value * NM + NM / 2) / 8 == m) stat(S, X); });
    });
  };
  using T1 = std::true_type; using T0 = std::false_type;
  using K0 = std::integral_constant<int, 0>; using K1 = std::integral_constant<int, 1>;
#define X_RGB 4
  using RGA = std::integral_constant<int, NM>;
  using RGB = std::integral_constant<int, NM - X_RGB>;

  a_m0(m0s);
  { const unsigned char* b0 = kbase(0); asm volatile("s_nop 0"); sfor<0, ND>([&](auto I) { dma(I, b0); }); }
  a_m0(m0s + G1_STAGE);
  { const unsigned char* b1 = kbase(1); asm volatile("s_nop 0"); sfor<0, ND>([&](auto I) { dma(I, b1); }); }
  a_waitv<ND>();
  a_bar();
  TCR(tc1_);
  {
    unsigned cB[2][2] = {{aB[0][0], aB[0][1]}, {aB[1][0], aB[1][1]}};
    sfor<0, NR>([&](auto J) { rd(J, K0{}, R0, aA, cB); });
    a_m0(m0s + 2 * G1_STAGE);
    { const unsigned char* b2 = kbase(2); asm volatile("s_nop 0"); sfor<0, ND>([&](auto I) { dma(I, b2); }); }
    block(K1{}, RGB{}, T1{}, T0{}, R0, R1, aA, cB, nullptr, 0);
  }
  unsigned so = 0;
  auto step = [&](int it, auto DO_DMA, auto NW) {
    const unsigned sn = so;
    so += G1_STAGE; so = so == 3 * G1_STAGE ? 0 : so;
    const unsigned cA = aA + so;
    const unsigned cB[2][2] = {{aB[0][0] + so, aB[0][1] + so}, {aB[1][0] + so, aB[1][1] + so}};
    const unsigned char* base = kbase(it + 2);
    a_waitv<decltype(NW)::value>();
    a_lgkm0();
    a_bar();
    block(K0{}, RGA{}, T0{}, DO_DMA, R1, R0, cA, cB, base, m0s + sn);
    block(K1{}, RGB{}, T1{}, T0{}, R0, R1, cA, cB, nullptr, 0);
  };
  for (int it = 1; it < 8; ++it) step(it, T1{}, std::integral_constant<int, ND>{});
  step(8, T0{}, std::integral_constant<int, ND>{});
  step(9, T0{}, std::integral_constant<int, 0>{});
  a_lgkm0();
  {
    const unsigned cB[2][2] = {{0, 0}, {0, 0}};
    block(K0{}, std::integral_constant<int, 0>{}, T0{}, T0{}, R1, R0, 0, cB, nullptr, 0);
  }
  asm volatile("s_nop 15\n\ts_nop 3" ::: "memory");
  TCR(tc2_);
  LAS float* red = (LAS float*)(sm + G1_RED);
  const float* cs = (const float*)(p.ws + WS_CS);
  f32x4 c1[NU], c3[NU];
#pragma unroll
  for (int i = 0; i < NU; ++i) {
    int un = u0 + UB + i; un = un < u0 + nu ? un : u0 + nu - 1;
    c1[i] = *(const GAS f32x4*)(cs + 16 * un + 4 * g);
    c3[i] = *(const GAS f32x4*)(cs + F + 16 * un + 4 * g);
  }
  {
    const float mean = xrow16(ssum) * (1.0f / D);
    const float var = fmaxf(xrow16(ssq) * (1.0f / D) - mean * mean, 0.f);
    const float sd = rbf(__builtin_amdgcn_sqrtf(rbf(rbf(var) + 1e-5f)));
    if (g == 0) *(LAS f32x2*)(red + 2 * (32 * tw + 16 * UH + r)) = (f32x2){rbf(mean), __builtin_amdgcn_rcpf(sd)};
  }
  BAR();
  unsigned char* HM = p.ws + WS_HM;
  float mt[2], rs[2];
#pragma unroll
  for (int t = 0; t < 2; ++t) { const f32x2 v = *(LAS f32x2*)(red + 2 * (32 * tw + 16 * t + r)); mt[t] = v.x; rs[t] = v.y; }
#pragma unroll
  for (int i = 0; i < NU; ++i) {
    if (UB + i < nu) {
      const int un = u0 + UB + i;
      v2u o[2];
#pragma unroll
      for (int t = 0; t < 2; ++t) {
        const float ra = rs[t] * -1.4426950408889634f, r2 = rs[t] * rs[t];
        float hv[4];
#pragma unroll
        for (int e = 0; e < 4; ++e) {
          const float a = acc[2 * i][t][e] - mt[t] * c1[i][e];
          const float bq = acc[2 * i + 1][t][e] - mt[t] * c3[i][e];
          const float sg = __builtin_amdgcn_rcpf(1.0f + __builtin_amdgcn_exp2f(a * ra));
          hv[e] = (a * bq) * (sg * r2);
        }
        o[t] = (v2u){pkbf(hv[0], hv[1]), pkbf(hv[2], hv[3])};
      }
      auto sx = __builtin_amdgcn_permlane16_swap(o[0].x, o[1].x, false, false);
      auto sy = __builtin_amdgcn_permlane16_swap(o[0].y, o[1].y, false, false);
      const v4u ov = {(unsigned)sx[0], (unsigned)sy[0], (unsigned)sx[1], (unsigned)sy[1]};
      const int ttile = 8 * rg + 2 * tw + (g & 1), chunk = 2 * (un & 1) + (g >> 1);
      *(GAS v4u*)(HM + ((size_t)ttile * 54 + (un >> 1)) * 1024 + chunk * 256 + 16 * r) = ov;
    }
  }
  TCR(tc3_);
  TCW(1, 10, tc0_); TCW(1, 11, tc1_); TCW(1, 12, tc2_); TCW(1, 13, tc3_);
}

__global__ __launch_bounds__(512, 2) void kG1(Params p) {
  extern __shared__ __attribute__((aligned(16))) char smem[];
  LAS char* sm = (LAS char*)smem;
  const int tid = threadIdx.x, lane = tid & 63, w = __builtin_amdgcn_readfirstlane(tid >> 6);
  const int b = blockIdx.x, xl = b & 7, j = b >> 3;
  const int rgi = j & 7, cgi = j >> 3;
  const int rg = 8 * (xl & 1) + rgi, cg = 4 * (xl >> 1) + cgi;
  const int u0 = cg < 12 ? 7 * cg : 84 + 6 * (cg - 12), nu = cg < 12 ? 7 : 6;
  const int kt0 = (rgi + cgi) % X_BLK1;
  TS(1, 0);
  if (w < 4) g1_wave<0>(p, sm, lane, w, rg, cg, u0, nu, kt0);
  else g1_wave<1>(p, sm, lane, w - 4, rg, cg, u0, nu, kt0);
  TS(1, 4);
}

#define X_BLK2 6
constexpr int G2_KS = 3;
constexpr int G2_NT = 54 / G2_KS;
constexpr int G2_STAGE = 9 * G2_KS * 1024;
constexpr int G2_NST = 5;
constexpr int G2_H = G2_NST * G2_STAGE;
constexpr int G2_LDS = G2_H + 80 * 128;

template <int DH> struct G2Set {
  v4u b[G2_KS];
  v4u a[DH ? 2 : 3][G2_KS];
};

template <int DH, int NDMA>
DI void g2_wave(const Params& p, LAS char* sm, int lane, int tt, int t0, int d0, int dg, int kt0) {
  constexpr int ND = DH ? 2 : 3, DB = DH ? 3 : 0, NR = G2_KS * (1 + ND), NM = G2_KS * ND, KS = G2_KS;
  TCDECL;
  TCR(tc0_);
  const unsigned smb = lds_addr(sm);
  const unsigned voff = 16 * lane;
  const unsigned char* gb = DH ? p.ws + WS_W2 + ((size_t)dg * 18 * 15 + 4 * tt) * 1024 : p.ws + WS_HM + (size_t)((t0 >> 4) + tt) * 54 * 1024;
  constexpr unsigned KSTR = DH ? 15 * 1024 : KS * 1024;
  const unsigned m0s = smb + (DH ? 4 * KS + 4 * tt : KS * tt) * 1024;
  auto kbase = [&](int it) {
    const int blk = it / X_BLK2;
    int k = it - blk * X_BLK2 + kt0; k = k >= X_BLK2 ? k - X_BLK2 : k;
    return gb + (size_t)((unsigned)(blk * X_BLK2 + k) * KSTR);
  };
  auto dma_tile = [&](int it, unsigned so) {
    const unsigned char* base = kbase(it);
    a_m0(m0s + so);
    asm volatile("s_nop 0");
    sfor<0, NDMA>([&](auto I) { a_dma_o<1024 * decltype(I)::value>(voff, base); });
  };
  const unsigned aB = smb + KS * tt * 1024 + 16 * lane, aA = smb + (4 * KS + DB * KS) * 1024 + 16 * lane;
  f32x4 acc[ND];
#pragma unroll
  for (int i = 0; i < ND; ++i) { acc[i] = (f32x4){0, 0, 0, 0}; a_pin(acc[i]); }
  G2Set<DH> R0, R1;
  auto rd = [&](auto J, G2Set<DH>& R, unsigned so) {
    constexpr int j = J;
    if constexpr (j < KS) a_dsr128<j * 1024>(R.b[j], aB + so);
    else { constexpr int a = (j - KS) / KS, ks = (j - KS) % KS; a_dsr128<(a * KS + ks) * 1024>(R.a[a][ks], aA + so); }
  };
  auto mm = [&](auto M, G2Set<DH>& R) {
    constexpr int m = M, ks = m / ND, a = m % ND;
    a_mfma(acc[a], R.a[a][ks], R.b[ks]);
  };
  if constexpr (DH == 0) {
    const unsigned char* hb = p.ws + WS_HT + ((size_t)d0 * L + t0) * 2;
    const unsigned ho = (unsigned)((lane >> 3) * L * 2 + (lane & 7) * 16);
    a_dma(smb + G2_H + tt * 1024, ho, hb + (size_t)tt * 8 * L * 2);
    a_dma(smb + G2_H + (tt + 4) * 1024, ho, hb + (size_t)(tt + 4) * 8 * L * 2);
    if (tt < 2) a_dma(smb + G2_H + (tt + 8) * 1024, ho, hb + (size_t)(tt + 8) * 8 * L * 2);
  }
#pragma unroll
  for (int i = 0; i < G2_NST - 1; ++i) dma_tile(i, i * G2_STAGE);
  auto waitv = [&](auto N) { a_waitv<decltype(N)::value>(); };
  auto step = [&](int it, unsigned so, unsigned sn, auto FIRST, auto DODMA, G2Set<DH>& X, G2Set<DH>& Y) {
    a_lgkm0();
    a_bar();
    if constexpr (FIRST) {
      sfor<0, NR>([&](auto J) { rd(J, Y, so); });
      if constexpr (DODMA) dma_tile(it + G2_NST - 1, sn);
    } else {
      sfor<0, NM>([&](auto M) {
        constexpr int m = M;
        mm(M, X);
        if constexpr (2 * m < NR) rd(std::integral_constant<int, 2 * m>{}, Y, so);
        if constexpr (2 * m + 1 < NR) rd(std::integral_constant<int, 2 * m + 1>{}, Y, so);
        if constexpr (m == NM - 1) {
          sfor<(2 * NM < NR ? 2 * NM : NR), NR>([&](auto J) { rd(J, Y, so); });
          if constexpr (DODMA) dma_tile(it + G2_NST - 1, sn);
        }
      });
    }
  };
  using T1 = std::true_type; using T0 = std::false_type;
  constexpr int INF = (G2_NST - 2) * NDMA;
  waitv(std::integral_constant<int, INF>{});
  TCR(tc1_);
  step(0, 0, (G2_NST - 1) * G2_STAGE, T1{}, T1{}, R1, R0);
  unsigned so = 0;
  auto nxt = [&](unsigned s) { s += G2_STAGE; return s == G2_NST * G2_STAGE ? 0u : s; };
  auto prv = [&](unsigned s) { return s == 0 ? (unsigned)((G2_NST - 1) * G2_STAGE) : s - G2_STAGE; };
  constexpr int NSTEADY = G2_NT - G2_NST;
  for (int it = 1; it + 1 <= NSTEADY; it += 2) {
    so = nxt(so);
    waitv(std::integral_constant<int, INF>{});
    step(it, so, prv(so), T0{}, T1{}, R0, R1);
    so = nxt(so);
    waitv(std::integral_constant<int, INF>{});
    step(it + 1, so, prv(so), T0{}, T1{}, R1, R0);
  }
  static_assert(NSTEADY == 13 && G2_NST == 5, "tail below is written for this shape");
  so = nxt(so); waitv(std::integral_constant<int, INF>{});       step(13, so, prv(so), T0{}, T1{}, R0, R1);
  so = nxt(so); waitv(std::integral_constant<int, INF>{});       step(14, so, 0, T0{}, T0{}, R1, R0);
  so = nxt(so); waitv(std::integral_constant<int, 2 * NDMA>{});  step(15, so, 0, T0{}, T0{}, R0, R1);
  so = nxt(so); waitv(std::integral_constant<int, NDMA>{});      step(16, so, 0, T0{}, T0{}, R1, R0);
  so = nxt(so); waitv(std::integral_constant<int, 0>{});         step(17, so, 0, T0{}, T0{}, R0, R1);
  a_lgkm0();
  sfor<0, NM>([&](auto M) { mm(M, R1); });
  asm volatile("s_nop 15\n\ts_nop 3" ::: "memory");
  TCR(tc2_);
  const int r = lane & 15, g = lane >> 4, qq = (lane & 15) >> 2, pp = lane & 3;
  v2u hv[ND];
#pragma unroll
  for (int i = 0; i < ND; ++i) a_dstr<0>(hv[i], smb + G2_H + (16 * (DB + i) + 4 * g + qq) * 128 + 32 * tt + 8 * pp);
  a_lgkm0();
#pragma unroll
  for (int i = 0; i < ND; ++i) a_pin(hv[i]);
#pragma unroll
  for (int i = 0; i < ND; ++i) {
    const float o0 = bflo(hv[i].x) + rbf(acc[i][0]), o1 = bfhi(hv[i].x) + rbf(acc[i][1]);
    const float o2 = bflo(hv[i].y) + rbf(acc[i][2]), o3 = bfhi(hv[i].y) + rbf(acc[i][3]);
    *(GAS v2u*)(p.out + (size_t)(t0 + 16 * tt + r) * D + d0 + 16 * (DB + i) + 4 * g) = (v2u){pkbf(o0, o1), pkbf(o2, o3)};
  }
  TCR(tc3_);
  TCW(2, 10, tc0_); TCW(2, 11, tc1_); TCW(2, 12, tc2_); TCW(2, 13, tc3_);
}

__global__ __launch_bounds__(512, 2) void kG2(Params p) {
  extern __shared__ __attribute__((aligned(16))) char smem[];
  LAS char* sm = (LAS char*)smem;
  const int tid = threadIdx.x, lane = tid & 63, w = __builtin_amdgcn_readfirstlane(tid >> 6);
  const int b = blockIdx.x, xl = b & 7, j = b >> 3;
  const int rgi = j & 7, dgi = j >> 3, xq = xl >> 1;
  const int rg = 8 * (xl & 1) + rgi, th = xq & 1, dg = 4 * (xq >> 1) + dgi;
  const int t0 = 128 * rg + 64 * th, d0 = 80 * dg;
  const int kt0 = (rgi + dgi) % X_BLK2;
  TS(2, 0);
  if (w < 4) g2_wave<0, 3>(p, sm, lane, w, t0, d0, dg, kt0);
  else if (w < 7) g2_wave<1, 4>(p, sm, lane, w - 4, t0, d0, dg, kt0);
  else g2_wave<1, 3>(p, sm, lane, 3, t0, d0, dg, kt0);
  TS(2, 4);
}

extern "C" void kernel_launch(void* const* d_in, const int* in_sizes, int n_in, void* d_out, int out_size, void* d_ws,
                              size_t ws_size, hipStream_t stream) {
  Params p;
  p.x = (const u16*)d_in[0];
  p.logA = (const u16*)d_in[1];
  p.Aim = (const u16*)d_in[2];
  p.Cre = (const u16*)d_in[3];
  p.Cim = (const u16*)d_in[4];
  p.logdt = (const u16*)d_in[5];
  p.Dsk = (const u16*)d_in[6];
  p.w1 = (const u16*)d_in[8];
  p.w3 = (const u16*)d_in[9];
  p.w2 = (const u16*)d_in[10];
  p.out = (u16*)d_out;
  p.ws = (unsigned char*)d_ws;
  (void)hipFuncSetAttribute((const void*)kA, hipFuncAttributeMaxDynamicSharedMemorySize, A_LDS);
  (void)hipFuncSetAttribute((const void*)kG1, hipFuncAttributeMaxDynamicSharedMemorySize, G1_LDS);
  (void)hipFuncSetAttribute((const void*)kG2, hipFuncAttributeMaxDynamicSharedMemorySize, G2_LDS);
  kA<<<256, 512, A_LDS, stream>>>(p);
  kG1<<<256, 512, G1_LDS, stream>>>(p);
  kG2<<<256, 512, G2_LDS, stream>>>(p);
}
```

```cpp
#include <hip/hip_runtime.h>
#include <hip/hip_bf16.h>
#include <stdint.h>
#include <type_traits>

#define GAS __attribute__((address_space(1)))
#define LAS __attribute__((address_space(3)))
#define DI __device__ __forceinline__

typedef unsigned short u16;
typedef __bf16 bf16x8 __attribute__((ext_vector_type(8)));
typedef __bf16 bf16x2 __attribute__((ext_vector_type(2)));
typedef _Float16 f16x8 __attribute__((ext_vector_type(8)));
typedef _Float16 f16x2 __attribute__((ext_vector_type(2)));
typedef short s16x4 __attribute__((ext_vector_type(4)));
typedef float f32x4 __attribute__((ext_vector_type(4)));
typedef float f32x2 __attribute__((ext_vector_type(2)));
typedef unsigned v4u __attribute__((ext_vector_type(4)));
typedef unsigned v2u __attribute__((ext_vector_type(2)));
typedef GAS unsigned gu32;
typedef GAS unsigned long long gu64;

constexpr int L = 2048, D = 640, NM = 128, F = 1728;

constexpr size_t WS_GEN = 0;
constexpr size_t WS_RDY = 128;
constexpr size_t WS_FLG = 1024;
constexpr size_t WS_CNT = 4096;
constexpr size_t WS_STATS = 20480;
constexpr size_t WS_CS = 36864;
constexpr size_t WS_HT = 65536;
constexpr size_t WS_W13 = WS_HT + (size_t)D * L * 2;
constexpr size_t WS_W2 = WS_W13 + (size_t)16 * 10 * 28 * 1024;
constexpr size_t WS_HM = WS_W2 + (size_t)8 * 18 * 15 * 1024;
constexpr size_t WS_END = WS_HM + (size_t)128 * 54 * 1024;

struct Params {
  const u16* x;
  const u16* logA;
  const u16* Aim;
  const u16* Cre;
  const u16* Cim;
  const u16* logdt;
  const u16* Dsk;
  const u16* w1;
  const u16* w3;
  const u16* w2;
  u16* out;
  unsigned char* ws;
};

DI float bf2f(unsigned b) { return __builtin_bit_cast(float, b << 16); }
DI float bflo(unsigned v) { return __builtin_bit_cast(float, v << 16); }
DI float bfhi(unsigned v) { return __builtin_bit_cast(float, v & 0xffff0000u); }
DI unsigned pkbf(float a, float b) {
  bf16x2 r = {(__bf16)a, (__bf16)b};
  return __builtin_bit_cast(unsigned, r);
}
DI unsigned pkh(float a, float b) {
  f16x2 r = {(_Float16)a, (_Float16)b};
  return __builtin_bit_cast(unsigned, r);
}
DI float rbf(float a) { return bflo(pkbf(a, a)); }
DI float hlo(unsigned v) { return (float)__builtin_bit_cast(f16x2, v)[0]; }
DI float hhi(unsigned v) { return (float)__builtin_bit_cast(f16x2, v)[1]; }

template <int CTRL> DI float dppf(float v) {
  return __builtin_bit_cast(float, __builtin_amdgcn_update_dpp(0, __builtin_bit_cast(int, v), CTRL, 0xf, 0xf, true));
}
DI float xrow16(float v) {
  unsigned u = __builtin_bit_cast(unsigned, v);
  auto a = __builtin_amdgcn_permlane16_swap(u, u, false, false);
  v = __builtin_bit_cast(float, (unsigned)a[0]) + __builtin_bit_cast(float, (unsigned)a[1]);
  u = __builtin_bit_cast(unsigned, v);
  auto b = __builtin_amdgcn_permlane32_swap(u, u, false, false);
  return __builtin_bit_cast(float, (unsigned)b[0]) + __builtin_bit_cast(float, (unsigned)b[1]);
}
DI float wave_sum(float v) {
  v += dppf<0x128>(v);
  v += dppf<0x124>(v);
  v += dppf<0x122>(v);
  v += dppf<0x121>(v);
  return xrow16(v);
}
template <int CTRL> DI unsigned dppu(unsigned v) { return (unsigned)__builtin_amdgcn_update_dpp(0, (int)v, CTRL, 0xf, 0xf, true); }
DI float wave_max_abs(unsigned u) {
  u = max(u, dppu<0x128>(u));
  u = max(u, dppu<0x124>(u));
  u = max(u, dppu<0x122>(u));
  u = max(u, dppu<0x121>(u));
  auto a = __builtin_amdgcn_permlane16_swap(u, u, false, false);
  u = max((unsigned)a[0], (unsigned)a[1]);
  auto b = __builtin_amdgcn_permlane32_swap(u, u, false, false);
  return __builtin_bit_cast(float, max((unsigned)b[0], (unsigned)b[1]));
}
DI f32x4 mfma_bf(v4u a, v4u b, f32x4 c) {
  return __builtin_amdgcn_mfma_f32_16x16x32_bf16(__builtin_bit_cast(bf16x8, a), __builtin_bit_cast(bf16x8, b), c, 0, 0, 0);
}
DI f32x4 mfma_h(v4u a, v4u b, f32x4 c) {
  return __builtin_amdgcn_mfma_f32_16x16x32_f16(__builtin_bit_cast(f16x8, a), __builtin_bit_cast(f16x8, b), c, 0, 0, 0);
}
DI v2u trread(LAS char* p) {
  s16x4 v = __builtin_amdgcn_ds_read_tr16_b64_v4i16((LAS s16x4*)p);
  return __builtin_bit_cast(v2u, v);
}
DI v4u ldsr128(LAS char* p) { return *(LAS v4u*)p; }
DI v2u ldsr64(LAS char* p) { return *(LAS v2u*)p; }
DI void glds16(const void* g, LAS char* l) {
  __builtin_amdgcn_global_load_lds((const GAS void*)g, (LAS void*)l, 16, 0, 0);
}
#define RLX_AGENT __ATOMIC_RELAXED, __HIP_MEMORY_SCOPE_AGENT
#define WAITV(n) asm volatile("s_waitcnt vmcnt(" #n ")" ::: "memory")
#define BAR() do { asm volatile("s_waitcnt lgkmcnt(0)" ::: "memory"); __builtin_amdgcn_s_barrier(); asm volatile("" ::: "memory"); } while (0)

struct Stamps {};
#define SC(st, i)
#define SR(st, i)
#define SCW(st)
DI f32x2 cmul(f32x2 a, f32x2 b) {
  f32x2 t, r;
  asm("v_pk_mul_f32 %0, %1, %2 op_sel_hi:[1,0]" : "=v"(t) : "v"(a), "v"(b));
  asm("v_pk_fma_f32 %0, %1, %2, %3 op_sel:[1,1,0] op_sel_hi:[0,1,1] neg_lo:[0,1,0]" : "=v"(r) : "v"(a), "v"(b), "v"(t));
  return r;
}
DI f32x2 unbf(unsigned v) { return (f32x2){bflo(v), bfhi(v)}; }
DI f32x2 fma2(f32x2 a, f32x2 b, f32x2 c) { return __builtin_elementwise_fma(a, b, c); }

template <int I, int N, class Fn> DI void sfor(Fn&& f) {
  if constexpr (I < N) { f(std::integral_constant<int, I>{}); sfor<I + 1, N>(f); }
}
DI void a_mfma(f32x4& c, const v4u& a, const v4u& b) {
  asm volatile("v_mfma_f32_16x16x32_bf16 %0, %1, %2, %0" : "+v"(c) : "v"(a), "v"(b));
}
DI void a_mfma_h(f32x4& c, const v4u& a, const v4u& b) {
  asm volatile("v_mfma_f32_16x16x32_f16 %0, %1, %2, %0" : "+v"(c) : "v"(a), "v"(b));
}
template <int OFF> DI void a_dsr64(v2u& d, unsigned addr) {
  asm volatile("ds_read_b64 %0, %1 offset:%2" : "=v"(d) : "v"(addr), "n"(OFF));
}
template <int OFF> DI void a_dsr128(v4u& d, unsigned addr) {
  asm volatile("ds_read_b128 %0, %1 offset:%2" : "=v"(d) : "v"(addr), "n"(OFF));
}
template <int OFF> DI void a_dstr(v2u& d, unsigned addr) {
  asm volatile("ds_read_b64_tr_b16 %0, %1 offset:%2" : "=v"(d) : "v"(addr), "n"(OFF));
}
DI void a_dma(unsigned lds, unsigned voff, const void* gbase) {
  asm volatile("s_mov_b32 m0, %0\n\ts_nop 0\n\tglobal_load_lds_dwordx4 %1, %2 sc1" ::"s"(lds), "v"(voff), "s"(gbase) : "memory");
}
template <int OFF> DI void a_ds_tid(unsigned v) { asm volatile("ds_write_addtid_b32 %0 offset:%1" ::"v"(v), "n"(OFF) : "memory"); }
DI void a_m0(unsigned lds) { asm volatile("s_mov_b32 m0, %0" ::"s"(lds) : "memory"); }
DI void a_m0n(unsigned lds) { asm volatile("s_mov_b32 m0, %0\n\ts_nop 0" ::"s"(lds) : "memory"); }
template <int OFF> DI void a_dma_o(unsigned voff, const void* gbase) {
  asm volatile("global_load_lds_dwordx4 %0, %1 offset:%2 sc1" ::"v"(voff), "s"(gbase), "n"(OFF) : "memory");
}
DI void a_dot_sq(float& c, unsigned v) { asm volatile("v_dot2c_f32_bf16 %0, %1, %1" : "+v"(c) : "v"(v)); }
DI void a_dot_one(float& c, unsigned v) { asm volatile("v_dot2c_f32_bf16 %0, 0x3f803f80, %1" : "+v"(c) : "v"(v)); }
template <int HI> DI void a_stat(float& sum, float& sq, unsigned v) {
  float x;
  if constexpr (HI)
    asm volatile("v_and_b32 %2, 0xffff0000, %3\n\tv_add_f32 %0, %0, %2\n\tv_fmac_f32 %1, %2, %2" : "+v"(sum), "+v"(sq), "=&v"(x) : "v"(v));
  else
    asm volatile("v_lshlrev_b32 %2, 16, %3\n\tv_add_f32 %0, %0, %2\n\tv_fmac_f32 %1, %2, %2" : "+v"(sum), "+v"(sq), "=&v"(x) : "v"(v));
}
template <int N> DI void a_waitl() { static_assert(N >= 0 && N <= 15, "lgkmcnt"); asm volatile("s_waitcnt lgkmcnt(%0)" ::"n"(N) : "memory"); }
template <int N> DI void a_waitv() { asm volatile("s_waitcnt vmcnt(%0)" ::"n"(N) : "memory"); }
template <class T> DI void a_pin(T& v) { asm volatile("" : "+v"(v)); }
DI void a_lgkm0() { asm volatile("s_waitcnt lgkmcnt(0)" ::: "memory"); }
DI void a_bar() { asm volatile("s_barrier" ::: "memory"); }
DI unsigned lds_addr(LAS char* p) { return (unsigned)(size_t)p; }

DI void ws_st128(const Params& p, unsigned off, v4u v) {
  __builtin_amdgcn_raw_buffer_store_b128(v, __builtin_amdgcn_make_buffer_rsrc(p.ws, 0, (unsigned)WS_END, 0x00020000), off, 0, 16);
}
constexpr unsigned SPIN_CAP = 1u << 18;
DI unsigned ws_ld(const Params& p, size_t off) { return __hip_atomic_load((gu32*)(p.ws + off), RLX_AGENT); }
DI void ws_st(const Params& p, size_t off, unsigned v) { __hip_atomic_store((gu32*)(p.ws + off), v, RLX_AGENT); }
DI void sync_all(const Params& p, int b, int lane, int w, unsigned tag) {
  unsigned rdy = 0;
  if (w == 0) rdy = ws_ld(p, WS_RDY);
  WAITV(0);
  a_bar();
  if (w == 0) {
    for (unsigned spin = 0; rdy != tag && spin < SPIN_CAP; ++spin) rdy = ws_ld(p, WS_RDY);
    if (lane < 8) __hip_atomic_fetch_add((gu32*)(p.ws + WS_CNT + (lane * 16 + (b >> 4)) * 128), 1u, RLX_AGENT);
    asm volatile("" ::: "memory");
    const size_t mine = WS_CNT + ((b & 7) * 16 + (lane & 15)) * 128;
    for (unsigned spin = 0; spin < SPIN_CAP; ++spin)
      if (__all(ws_ld(p, mine) == 16u)) break;
    if (b == 0 && lane == 0) ws_st(p, WS_GEN, tag);
  }
  a_bar();
}
DI void group_arrive(const Params& p, int rg, int cg, int tid, unsigned tag) {
  WAITV(0);
  a_bar();
  if (tid == 0) ws_st(p, WS_FLG + rg * 128 + cg * 4, tag);
}
DI void group_wait(const Params& p, int rg, int lane, int w, unsigned tag) {
  if (w == 0) {
    for (unsigned spin = 0; spin < SPIN_CAP; ++spin)
      if (__all(ws_ld(p, WS_FLG + rg * 128 + (lane & 15) * 4) == tag)) break;
  }
  a_bar();
}

constexpr int T_CH = 1344;
constexpr int A_T = 0;
constexpr int A_SC = 4 * T_CH;
constexpr int KG_ROW = 272;
constexpr int KG_GA = 21 * KG_ROW, KG_EB = 32 * KG_ROW;
constexpr int A_KG = A_SC + 64;
constexpr int A_GA = A_KG, A_EB = A_GA + 8 * KG_GA;
static_assert(7 * KG_EB < 65536 && A_EB + 31 * KG_ROW < 65536, "ds_write_addtid: M0 and offset are 16 bits each");
constexpr int KC_COPY = 4416, KC_CH = 4 * KC_COPY;
constexpr int A_EX = A_KG + 4 * KC_CH;
constexpr int A_U = A_EB + 8 * KG_EB;
constexpr int U_CH = 4608;
constexpr int A_X = A_U + 4 * U_CH;
constexpr int A_PX = A_X;
constexpr int A_LDS = A_X + 4 * 4096;
static_assert(A_EX + 8 * 4096 <= A_U, "lds map");
static_assert(A_LDS <= 163840 && A_KG % 64 == 0, "lds");

struct ExpTab {
  unsigned v[256 * 12];
  constexpr ExpTab() : v() {
    for (int c = 0; c < 256; ++c)
      for (int i = 0; i < 12; ++i) {
        const int l = 2051 - 8 * c - (i < 11 ? i : 10);
        int sh = 0;
        while ((l >> sh) >= 256) ++sh;
        int idx = l;
        if (sh > 0) idx = 128 * sh + ((l + (1 << (sh - 1)) - 1 + ((l >> sh) & 1)) >> sh);
        v[c * 12 + i] = 2u * (unsigned)idx;
      }
  }
};
__device__ const ExpTab g_exp_tab = ExpTab();

template <int OFF> DI void g_ld32(unsigned& d, unsigned voff, const void* base) {
  asm volatile("global_load_dword %0, %1, %2 offset:%3" : "=v"(d) : "v"(voff), "s"(base), "n"(OFF) : "memory");
}
DI void g_ld32_sc1(unsigned& d, unsigned voff, const void* base) {
  asm volatile("global_load_dword %0, %1, %2 sc1" : "=v"(d) : "v"(voff), "s"(base) : "memory");
}
DI void g_ld16(unsigned& d, unsigned voff, const void* base) {
  asm volatile("global_load_ushort %0, %1, %2" : "=v"(d) : "v"(voff), "s"(base) : "memory");
}
template <int OFF> DI void g_ld128(v4u& d, unsigned voff, const void* base) {
  asm volatile("global_load_dwordx4 %0, %1, %2 offset:%3" : "=v"(d) : "v"(voff), "s"(base), "n"(OFF) : "memory");
}
template <int OFF> DI void g_ld128_sc1(v4u& d, unsigned voff, const void* base) {
  asm volatile("global_load_dwordx4 %0, %1, %2 offset:%3 sc1" : "=v"(d) : "v"(voff), "s"(base), "n"(OFF) : "memory");
}
template <int OFF> DI void g_ld64(v2u& d, unsigned voff, const void* base) {
  asm volatile("global_load_dwordx2 %0, %1, %2 offset:%3" : "=v"(d) : "v"(voff), "s"(base), "n"(OFF) : "memory");
}

DI void ln_rows(const Params& p, int row0, int lane, unsigned tag) {
  const int row = row0 + (lane >> 4);
  v4u xr[5];
#pragma unroll
  for (int i = 0; i < 5; ++i) xr[i] = *(const GAS v4u*)(p.x + (size_t)row * D + 8 * ((lane & 15) + 16 * i));
  f32x2 s2[2] = {{0, 0}, {0, 0}}, q2[2] = {{0, 0}, {0, 0}};
#pragma unroll
  for (int i = 0; i < 5; ++i)
#pragma unroll
    for (int e = 0; e < 4; ++e) {
      const unsigned d = xr[i][e];
      const f32x2 v = unbf(d);
      s2[e & 1] += v;
      q2[e & 1] = fma2(v, v, q2[e & 1]);
    }
  float sm_ = (s2[0].x + s2[0].y) + (s2[1].x + s2[1].y), sq = (q2[0].x + q2[0].y) + (q2[1].x + q2[1].y);
  sm_ += dppf<0x128>(sm_); sq += dppf<0x128>(sq);
  sm_ += dppf<0x124>(sm_); sq += dppf<0x124>(sq);
  sm_ += dppf<0x122>(sm_); sq += dppf<0x122>(sq);
  sm_ += dppf<0x121>(sm_); sq += dppf<0x121>(sq);
  const float mean = sm_ * (1.0f / D);
  const float var = fmaxf(sq * (1.0f / D) - mean * mean, 0.f);
  const float sd = rbf(__builtin_amdgcn_sqrtf(rbf(rbf(var) + 1e-5f)));
  if ((lane & 15) == 0)
    __hip_atomic_store((gu64*)(p.ws + WS_STATS) + row, ((unsigned long long)tag << 32) | pkbf(mean, sd), RLX_AGENT);
}

template <int HALF>
DI void conv_tail(const Params& p, LAS char* sm, f32x4 (&acc)[8], int lane, int w, int ch, int c0, unsigned vdsk, Stamps& stp) {
  const int r = lane & 15, q = lane >> 4;
  LAS char* mine = sm + A_EX + w * 4096 + 16 * lane;
  LAS char* other = sm + A_EX + (w ^ 4) * 4096 + 16 * lane;
#pragma unroll
  for (int i = 0; i < 4; ++i) *(LAS f32x4*)(mine + 1024 * i) = acc[4 * (1 - HALF) + i];
  LAS char* up = sm + A_U + ch * U_CH + 512 + 2048 * HALF + 32 * r + 8 * q;
  LAS char* xp = sm + A_X + ch * 4096 + 2048 * HALF + 32 * r + 8 * q;
  v2u uv[4], xv[4];
#pragma unroll
  for (int i = 0; i < 4; ++i) { uv[i] = ldsr64(up + 512 * i); xv[i] = ldsr64(xp + 512 * i); }
  const float scale = *(LAS float*)(sm + A_SC + 4 * ch);
  const float dsk = bf2f(vdsk);
  f32x2 base[4][2];
#pragma unroll
  for (int i = 0; i < 4; ++i) {
    base[i][0] = (f32x2){__builtin_fmaf(hlo(uv[i].x), dsk, bflo(xv[i].x)), __builtin_fmaf(hhi(uv[i].x), dsk, bfhi(xv[i].x))};
    base[i][1] = (f32x2){__builtin_fmaf(hlo(uv[i].y), dsk, bflo(xv[i].y)), __builtin_fmaf(hhi(uv[i].y), dsk, bfhi(xv[i].y))};
  }
  BAR();
  const f32x2 sc2 = {scale, scale};
  v2u ho[4];
#pragma unroll
  for (int i = 0; i < 4; ++i) {
    const f32x4 y = acc[4 * HALF + i] + *(LAS f32x4*)(other + 1024 * i);
    const f32x2 h0 = fma2((f32x2){y[0], y[1]}, sc2, base[i][0]), h1 = fma2((f32x2){y[2], y[3]}, sc2, base[i][1]);
    ho[i] = (v2u){pkbf(h0.x, h0.y), pkbf(h1.x, h1.y)};
  }
  SC(stp, 8);
  const unsigned ho_ = (unsigned)WS_HT + 2 * ((c0 + ch) * L + 1024 * HALF + 256 * (q & 1) + 16 * r + 8 * (q >> 1));
#pragma unroll
  for (int i = 0; i < 4; i += 2) {
    auto sx = __builtin_amdgcn_permlane16_swap(ho[i].x, ho[i + 1].x, false, false);
    auto sy = __builtin_amdgcn_permlane16_swap(ho[i].y, ho[i + 1].y, false, false);
    ws_st128(p, ho_ + 512 * i, (v4u){(unsigned)sx[0], (unsigned)sy[0], (unsigned)sx[1], (unsigned)sy[1]});
  }
}

struct ConvSet { v2u al[8], ah[8]; v4u b[8]; };

DI unsigned conv_role(const Params& p, LAS char* sm, int tid, int lane, int w, int cgp, Stamps& stp) {
  const int c0 = 4 * cgp;
  const unsigned smb = lds_addr(sm);
  const int kch = c0 + (w >> 1), hf = w & 1, n = 64 * hf + lane;
  const int ec = tid & 255, epair = tid >> 8;
  unsigned gen, vdt, vla, vai, vcr, vci, vcr2, vci2, vdsk;
  v4u et[3];
  v2u xg[4];
  const unsigned go = (unsigned)(4 * tid) * (D * 2);
  const u16* xc = p.x + c0;
  {
    const unsigned zero = 0, po = 2 * (kch * NM + n), po2 = 2 * (kch * NM + (n ^ 64));
    g_ld16(vdt, zero, p.logdt + kch);
    g_ld16(vla, po, p.logA); g_ld16(vai, po, p.Aim); g_ld16(vcr, po, p.Cre); g_ld16(vci, po, p.Cim);
    g_ld16(vcr2, po2, p.Cre); g_ld16(vci2, po2, p.Cim);
    g_ld32_sc1(gen, zero, p.ws + WS_GEN);
    g_ld16(vdsk, zero, p.Dsk + c0 + (w & 3));
    g_ld128<0>(et[0], 48 * ec, g_exp_tab.v); g_ld128<16>(et[1], 48 * ec, g_exp_tab.v); g_ld128<32>(et[2], 48 * ec, g_exp_tab.v);
    asm volatile("s_barrier" ::: "memory");
    g_ld64<0>(xg[0], go, xc); g_ld64<1280>(xg[1], go, xc);
  }
  SC(stp, 10);
  asm volatile("s_waitcnt vmcnt(2)"
               : "+v"(gen), "+v"(vdt), "+v"(vla), "+v"(vai), "+v"(vcr), "+v"(vci), "+v"(vcr2), "+v"(vci2), "+v"(vdsk), "+v"(et[0]),
                 "+v"(et[1]), "+v"(et[2])::"memory");
  const unsigned tag = gen + 1u;
  SC(stp, 1);
  float kscale;
  {
    const float dt = rbf(__builtin_amdgcn_exp2f(bf2f(vdt) * 1.4426950408889634f));
    const float are = -rbf(__builtin_amdgcn_exp2f(bf2f(vla) * 1.4426950408889634f)) * dt, aim = bf2f(vai) * dt;
    const float cmax = wave_max_abs(max(max((vcr & 0x7fffu) << 16, (vci & 0x7fffu) << 16), max((vcr2 & 0x7fffu) << 16, (vci2 & 0x7fffu) << 16)));
    const float cinv = cmax > 0.f ? __builtin_amdgcn_rcpf(cmax) : 0.f;
    kscale = 2.0f * dt * cmax;
    const float al = are * 1.4426950408889634f, ar = aim * 0.15915494309189535f;
    auto cpow = [&](float pw) -> f32x2 {
      const float e = __builtin_amdgcn_exp2f(al * pw);
      const float f = __builtin_amdgcn_fractf(ar * pw);
      return (f32x2){e * __builtin_amdgcn_cosf(f), e * __builtin_amdgcn_sinf(f)};
    };
    const f32x2 w1 = cpow(1.f), w8 = cpow(8.f), w16 = cpow(16.f), w32 = cpow(32.f), W1 = cpow(256.f);
    f32x2 e[4] = {{1.f, 0.f}, w8, w16, cmul(w8, w16)};
    a_m0n(smb + w * KG_EB);
    sfor<0, 8>([&](auto J) {
      constexpr int j = 4 * decltype(J)::value;
      unsigned pk[4];
#pragma unroll
      for (int i = 0; i < 4; ++i) pk[i] = pkh(e[i].x, e[i].y);
      a_ds_tid<A_EB + (j + 0) * KG_ROW>(pk[0]); a_ds_tid<A_EB + (j + 1) * KG_ROW>(pk[1]);
      a_ds_tid<A_EB + (j + 2) * KG_ROW>(pk[2]); a_ds_tid<A_EB + (j + 3) * KG_ROW>(pk[3]);
      if constexpr (j + 4 < 32) {
#pragma unroll
        for (int i = 0; i < 4; ++i) e[i] = cmul(e[i], w32);
      }
    });
    g_ld64<2560>(xg[2], go, xc);
    const f32x2 v1 = {w1.x, -w1.y}, V1 = {W1.x, -W1.y};
    const f32x2 v2 = cmul(v1, v1), v4 = cmul(v2, v2);
    f32x2 g[21];
    g[0] = (f32x2){bf2f(vcr) * cinv, -bf2f(vci) * cinv};
    g[1] = cmul(g[0], v1); g[2] = cmul(g[0], v2); g[3] = cmul(g[1], v2);
#pragma unroll
    for (int r = 4; r < 8; ++r) g[r] = cmul(g[r - 4], v4);
    const f32x2 V2 = cmul(V1, V1), V3 = cmul(V2, V1), V4 = cmul(V2, V2);
#pragma unroll
    for (int bb = 0; bb < 4; ++bb) g[8 + bb] = cmul(g[2 * bb], V1);
    g[12] = cmul(g[0], V2); g[13] = cmul(g[4], V2); g[14] = cmul(g[0], V3); g[15] = cmul(g[4], V3);
    g[16] = cmul(g[0], V4);
    g[17] = cmul(g[16], V1); g[18] = cmul(g[16], V2); g[19] = cmul(g[16], V3); g[20] = cmul(g[16], V4);
    a_m0n(smb + w * KG_GA);
    sfor<0, 21>([&](auto R) { constexpr int rw = R; a_ds_tid<A_GA + rw * KG_ROW>(pkh(g[rw].x, g[rw].y)); });
    g_ld64<3840>(xg[3], go, xc);
  }
  f32x4 d0, d1;
  {
    const int r = lane & 15, q = lane >> 4;
    f32x4 d[2][2];
#pragma unroll
    for (int i = 0; i < 4; ++i) { d[i >> 1][i & 1] = (f32x4){0, 0, 0, 0}; a_pin(d[i >> 1][i & 1]); }
    a_lgkm0();
    const unsigned ga = smb + A_GA + w * KG_GA + r * KG_ROW + 16 * q, eb = smb + A_EB + w * KG_EB + r * KG_ROW + 16 * q;
    v4u a[2][4], bb[2][4];
    sfor<0, 4>([&](auto S) {
      constexpr int s = S;
      a_dsr128<64 * s>(a[0][s], ga); a_dsr128<64 * s + 16 * KG_ROW>(a[1][s], ga);
      a_dsr128<64 * s>(bb[0][s], eb); a_dsr128<64 * s + 16 * KG_ROW>(bb[1][s], eb);
    });
    a_lgkm0();
#pragma unroll
    for (int s = 0; s < 4; ++s)
#pragma unroll
      for (int i = 0; i < 4; ++i) a_mfma_h(d[i >> 1][i & 1], a[i >> 1][s], bb[i & 1][s]);
    asm volatile("s_nop 15" ::: "memory");
#pragma unroll
    for (int i = 0; i < 4; ++i) a_pin(d[i >> 1][i & 1]);
    LAS char* px = sm + A_PX + w * 2048 + 16 * lane;
    if (tid == 128 * (w >> 1)) *(LAS float*)(sm + A_SC + 4 * (w >> 1)) = kscale;
    if (hf == 0) {
      *(LAS f32x4*)px = d[0][1]; *(LAS f32x4*)(px + 1024) = d[1][1]; d0 = d[0][0]; d1 = d[1][0];
    } else {
      *(LAS f32x4*)px = d[0][0]; *(LAS f32x4*)(px + 1024) = d[1][0]; d0 = d[0][1]; d1 = d[1][1];
    }
  }
  SC(stp, 2);
  BAR();
  {
    const int r = lane & 15, q = lane >> 4;
    LAS char* po = sm + A_PX + (w ^ 1) * 2048 + 16 * lane;
    d0 += *(LAS f32x4*)po;
    d1 += *(LAS f32x4*)(po + 1024);
    const int j = 16 * hf + r;
    LAS char* T = sm + A_T + (w >> 1) * T_CH;
    const int ib = q < 2 ? 4 * q + 8 * j : (q == 2 ? 256 + 4 * j : 384 + 2 * j);
    const int is2 = q == 3 ? 64 : 2;
    *(LAS unsigned*)(T + 2 * ib) = pkh(d0[0], d0[1]);
    *(LAS unsigned*)(T + 2 * (ib + is2)) = pkh(d0[2], d0[3]);
#pragma unroll
    for (int i = 0; i < 4; ++i) {
      const int i1 = q == 0 ? 512 + 32 * i + j : ((q == 1 && i == 0 && j == 0) ? 640 : 641 + i);
      *(LAS _Float16*)(T + 2 * i1) = (_Float16)d1[i];
    }
  }
  SC(stp, 3);
  BAR();
  v4u sg[2];
  g_ld128_sc1<0>(sg[0], 32 * tid, p.ws + WS_STATS);
  g_ld128_sc1<16>(sg[1], 32 * tid, p.ws + WS_STATS);
  {
    const unsigned eo[11] = {et[0].x, et[0].y, et[0].z, et[0].w, et[1].x, et[1].y, et[1].z, et[1].w, et[2].x, et[2].y, et[2].z};
    typedef unsigned short u16x2 __attribute__((ext_vector_type(2)));
#pragma unroll
    for (int cc = 0; cc < 2; ++cc) {
      const int ch = 2 * epair + cc;
      LAS char* T = sm + A_T + ch * T_CH;
      unsigned d[6];
#pragma unroll
      for (int i = 0; i < 5; ++i) {
        const u16x2 pr = {*(LAS u16*)(T + eo[2 * i]), *(LAS u16*)(T + eo[2 * i + 1])};
        d[i] = __builtin_bit_cast(unsigned, pr);
      }
      d[5] = *(LAS u16*)(T + eo[10]);
      LAS char* K = sm + A_KG + ch * KC_CH + 16 * ec;
      v4u c3 = {d[0], d[1], d[2], d[3]};
      v4u c1 = {d[1], d[2], d[3], d[4]};
      v4u c2, c0v;
#pragma unroll
      for (int i = 0; i < 4; ++i) {
        c2[i] = __builtin_amdgcn_alignbit(d[i + 1], d[i], 16);
        c0v[i] = __builtin_amdgcn_alignbit(d[i + 2], d[i + 1], 16);
      }
      *(LAS v4u*)(K + 0 * KC_COPY) = c0v;
      *(LAS v4u*)(K + 1 * KC_COPY) = c1;
      *(LAS v4u*)(K + 2 * KC_COPY) = c2;
      *(LAS v4u*)(K + 3 * KC_COPY) = c3;
    }
    if (tid < 256) {
      const int ch = tid >> 6, pp = (tid >> 4) & 3, i = tid & 15;
      const int l = pp - i;
      const u16 val = l >= 0 ? *(LAS u16*)(sm + A_T + ch * T_CH + 2 * l) : (u16)0;
      *(LAS u16*)(sm + A_KG + ch * KC_CH + pp * KC_COPY + 2 * (2048 + i)) = val;
    }
    if (tid >= 384) {
      v4u z = {0, 0, 0, 0};
      *(LAS v4u*)(sm + A_U + ((tid - 384) >> 5) * U_CH + 16 * (tid & 31)) = z;
    }
  }
  SC(stp, 4);
  {
    asm volatile("s_waitcnt vmcnt(0)" : "+v"(sg[0]), "+v"(sg[1]), "+v"(xg[0]), "+v"(xg[1]), "+v"(xg[2]), "+v"(xg[3])::"memory");
    for (unsigned spin = 0; spin < (1u << 20); ++spin) {
      if (__all(sg[0].y == tag && sg[0].w == tag && sg[1].y == tag && sg[1].w == tag)) break;
      __builtin_amdgcn_s_sleep(1);
      g_ld128_sc1<0>(sg[0], 32 * tid, p.ws + WS_STATS);
      g_ld128_sc1<16>(sg[1], 32 * tid, p.ws + WS_STATS);
      asm volatile("s_waitcnt vmcnt(0)" : "+v"(sg[0]), "+v"(sg[1])::"memory");
    }
    SC(stp, 5);
    f32x2 ua[4], ub[4];
#pragma unroll
    for (int k = 0; k < 4; ++k) {
      const unsigned st = k == 0 ? sg[0].x : k == 1 ? sg[0].z : k == 2 ? sg[1].x : sg[1].z;
      const float rs = __builtin_amdgcn_rcpf(bfhi(st)), mr = -bflo(st) * rs;
      ua[k] = fma2(unbf(xg[k].x), (f32x2){rs, rs}, (f32x2){mr, mr});
      ub[k] = fma2(unbf(xg[k].y), (f32x2){rs, rs}, (f32x2){mr, mr});
    }
    LAS char* ud = sm + A_U + 512 + 8 * tid;
    *(LAS v2u*)(ud + 0 * U_CH) = (v2u){pkh(ua[0].x, ua[1].x), pkh(ua[2].x, ua[3].x)};
    *(LAS v2u*)(ud + 1 * U_CH) = (v2u){pkh(ua[0].y, ua[1].y), pkh(ua[2].y, ua[3].y)};
    *(LAS v2u*)(ud + 2 * U_CH) = (v2u){pkh(ub[0].x, ub[1].x), pkh(ub[2].x, ub[3].x)};
    *(LAS v2u*)(ud + 3 * U_CH) = (v2u){pkh(ub[0].y, ub[1].y), pkh(ub[2].y, ub[3].y)};
    const unsigned s0 = 0x05040100u, s1 = 0x07060302u;
    LAS char* xd = sm + A_X + 8 * tid;
    *(LAS v2u*)(xd + 0 * 4096) = (v2u){__builtin_amdgcn_perm(xg[1].x, xg[0].x, s0), __builtin_amdgcn_perm(xg[3].x, xg[2].x, s0)};
    *(LAS v2u*)(xd + 1 * 4096) = (v2u){__builtin_amdgcn_perm(xg[1].x, xg[0].x, s1), __builtin_amdgcn_perm(xg[3].x, xg[2].x, s1)};
    *(LAS v2u*)(xd + 2 * 4096) = (v2u){__builtin_amdgcn_perm(xg[1].y, xg[0].y, s0), __builtin_amdgcn_perm(xg[3].y, xg[2].y, s0)};
    *(LAS v2u*)(xd + 3 * 4096) = (v2u){__builtin_amdgcn_perm(xg[1].y, xg[0].y, s1), __builtin_amdgcn_perm(xg[3].y, xg[2].y, s1)};
  }
  const int ch = w & 3, half = w >> 2, r = lane & 15, q = lane >> 4;
  f32x4 acc[8];
#pragma unroll
  for (int i = 0; i < 8; ++i) { acc[i] = (f32x4){0, 0, 0, 0}; a_pin(acc[i]); }
  const unsigned aA = smb + A_KG + ch * KC_CH + (r & 3) * KC_COPY + 2 * (2048 - 16 - 4 * (r >> 2) + 8 * q) - 512 * 7 - 64 * (4 * half + 3);
  const unsigned aB = smb + A_U + ch * U_CH + 512 + 32 * r - 32 + 16 * q - 64 * (4 * half + 3);
  SC(stp, 6);
  BAR();
  {
    ConvSet R0, R1;
    auto rd = [&](auto J, auto DD, ConvSet& R) {
      constexpr int j = J, dd = DD;
      if constexpr (j >= 2 && j < 10) a_dsr128<512 * (j - 2) + 64 * (3 - dd)>(R.b[j - 2], aB);
      else {
        constexpr int jj = j < 2 ? j : j - 8, g = jj >> 1;
        if constexpr (jj & 1) a_dsr64<512 * (7 - g) + 64 * (3 - dd) + 8>(R.ah[g], aA);
        else a_dsr64<512 * (7 - g) + 64 * (3 - dd)>(R.al[g], aA);
      }
    };
    auto block = [&](auto DD, ConvSet& X, ConvSet& Y) {
      constexpr int dd = DD;
      sfor<0, 8>([&](auto G) {
        constexpr int g = G, m0 = g * 8 - g * (g - 1) / 2;
        const v4u a = {X.al[g].x, X.al[g].y, X.ah[g].x, X.ah[g].y};
        sfor<g, 8>([&](auto N) {
          constexpr int nn = N, m = m0 + nn - g;
          if constexpr (dd == 0) {
            constexpr int need = g == 0 ? 2 + nn : 9 + 2 * g, mine = m < 24 ? m : 24, out = 23 - need + mine;
            if constexpr (g == 0 ? nn == 7 : nn == g) a_waitl<(out < 15 ? out : 15)>();
          }
          a_mfma_h(acc[nn], a, X.b[nn - g]);
          if constexpr (dd < 3 && m < 24) rd(std::integral_constant<int, m>{}, std::integral_constant<int, dd + 1>{}, Y);
        });
      });
      if constexpr (dd < 3) a_lgkm0();
    };
    sfor<0, 24>([&](auto J) { rd(J, std::integral_constant<int, 0>{}, R0); });
    a_waitl<15>();
    block(std::integral_constant<int, 0>{}, R0, R1);
    block(std::integral_constant<int, 1>{}, R1, R0);
    block(std::integral_constant<int, 2>{}, R0, R1);
    block(std::integral_constant<int, 3>{}, R1, R0);
    asm volatile("s_nop 15\n\ts_nop 3" ::: "memory");
#pragma unroll
    for (int i = 0; i < 8; ++i) a_pin(acc[i]);
  }
  SC(stp, 7);
  if (half == 0) conv_tail<0>(p, sm, acc, lane, w, ch, c0, vdsk, stp);
  else conv_tail<1>(p, sm, acc, lane, w, ch, c0, vdsk, stp);
  SC(stp, 9);
  return tag;
}

DI void helper_role(const Params& p, LAS char* sm, int lane, int w, int hidx) {
  const int g = lane >> 4, qq = (lane & 15) >> 2, pp = lane & 3;
  if (hidx < 54) {
    const int mat = hidx / 27, cb = hidx % 27;
    const u16* W = mat ? p.w3 : p.w1;
    LAS char* buf = sm + w * 12288;
#pragma unroll
    for (int i = 0; i < 3; ++i) {
      const int s = w + 8 * i;
      if (s < 20) {
#pragma unroll
        for (int k = 0; k < 4; ++k)
          glds16(W + (size_t)(32 * s + 8 * k + (lane >> 3)) * F + 64 * cb + 8 * (lane & 7), buf + i * 4096 + k * 1024);
      }
    }
    WAITV(0);
    float cs[4] = {0, 0, 0, 0};
#pragma unroll
    for (int i = 0; i < 3; ++i) {
      const int s = w + 8 * i;
      if (s < 20) {
#pragma unroll
        for (int u = 0; u < 4; ++u) {
          LAS char* a = buf + i * 4096 + (8 * g + qq) * 128 + u * 32 + pp * 8;
          const v2u lo = trread(a), hi = trread(a + 4 * 128);
          const v4u fr = {lo.x, lo.y, hi.x, hi.y};
          const int un = 4 * cb + u, cgu = un < 84 ? un / 7 : 12 + (un - 84) / 6, ul = un < 84 ? un % 7 : (un - 84) % 6;
          ws_st128(p, (unsigned)WS_W13 + ((cgu * 10 + (s >> 1)) * 28 + 4 * ul + 2 * mat + (s & 1)) * 1024 + 16 * lane, fr);
          cs[u] += (bflo(fr.x) + bfhi(fr.x)) + (bflo(fr.y) + bfhi(fr.y)) + (bflo(fr.z) + bfhi(fr.z)) + (bflo(fr.w) + bfhi(fr.w));
        }
      }
    }
    LAS float* red = (LAS float*)(sm + 98304);
#pragma unroll
    for (int u = 0; u < 4; ++u) {
      const float t = xrow16(cs[u]);
      if (lane < 16) red[w * 64 + u * 16 + lane] = t;
    }
    __syncthreads();
    if (w == 0) {
      float t = 0;
#pragma unroll
      for (int i = 0; i < 8; ++i) t += red[i * 64 + lane];
      ws_st(p, WS_CS + 4 * (mat * F + 64 * cb + lane), __builtin_bit_cast(unsigned, t));
    }
  } else {
    LAS char* buf = sm + w * 8192;
#pragma unroll
    for (int i = 0; i < 2; ++i) {
      const int blk = (hidx - 54) * 8 + w + 336 * i;
      if (blk < 540) {
        const int s = blk / 10, cb = blk % 10;
#pragma unroll
        for (int k = 0; k < 4; ++k)
          glds16(p.w2 + (size_t)(32 * s + 8 * k + (lane >> 3)) * D + 64 * cb + 8 * (lane & 7), buf + i * 4096 + k * 1024);
      }
    }
    WAITV(0);
#pragma unroll
    for (int i = 0; i < 2; ++i) {
      const int blk = (hidx - 54) * 8 + w + 336 * i;
      if (blk < 540) {
        const int s = blk / 10, cb = blk % 10;
#pragma unroll
        for (int u = 0; u < 4; ++u) {
          LAS char* a = buf + i * 4096 + (8 * g + qq) * 128 + u * 32 + pp * 8;
          const v2u lo = trread(a), hi = trread(a + 4 * 128);
          const v4u fr = {lo.x, lo.y, hi.x, hi.y};
          const int dt = 4 * cb + u;
          ws_st128(p, (unsigned)WS_W2 + (((dt / 5) * 18 + s / 3) * 15 + (dt % 5) * 3 + s % 3) * 1024 + 16 * lane, fr);
        }
      }
    }
  }
}

#define X_BLK1 1
constexpr int G1_STAGE = 16384 + 28 * 1024;
constexpr int G1_RED = 148480;
constexpr int G1_CS = G1_RED + 1024;
constexpr int G1_LDS = G1_CS + 1024;
static_assert(3 * G1_STAGE <= G1_RED, "lds map");

template <int UH> struct G1Set {
  v4u a[UH ? 6 : 8];
  v2u b[2][2];
};

template <int UH, class Hook>
DI void g1_wave(const Params& p, LAS char* sm, int lane, int tw, int rg, int cg, int u0, int nu, int kt0, Stamps& stp, Hook&& after_loop) {
  constexpr int NU = UH ? 3 : 4, UB = UH ? 4 : 0, NA = 2 * NU, NR = NA + 4, NM = 2 * NA, ND = UH ? 7 : 4;
  const unsigned smb = lds_addr(sm);
  const int g = lane >> 4, qq = (lane & 15) >> 2, pp = lane & 3, r = lane & 15;
  unsigned voff[UH ? 1 : 4];
  const unsigned char* gb;
  constexpr unsigned KSTR = UH ? 28 * 1024 : 64 * L * 2;
  unsigned m0s;
  if constexpr (UH == 0) {
#pragma unroll
    for (int i = 0; i < 4; ++i) {
      const int row = 16 * tw + 4 * i + (lane >> 4);
      const int ch = (lane & 15) ^ (((row & 3) << 2) | ((row >> 2) & 3));
      voff[i] = (unsigned)(row * L * 2 + ch * 16 - 1024 * i);
    }
    gb = p.ws + WS_HT + 256 * rg;
    m0s = smb + 4096 * tw;
  } else {
    voff[0] = 16 * lane;
    gb = p.ws + WS_W13 + ((size_t)cg * 280 + 7 * tw + 3) * 1024;
    m0s = smb + 16384 + (7 * tw + 3) * 1024;
  }
  auto dma = [&](auto I, const unsigned char* base) {
    constexpr int i = I;
    if constexpr (UH == 0) a_dma_o<1024 * i>(voff[i], base);
    else a_dma_o<1024 * (i - 3)>(voff[0], base);
  };
  auto kbase = [&](int it) {
    const int blk = it / X_BLK1;
    int k = it - blk * X_BLK1 + kt0; k = k >= X_BLK1 ? k - X_BLK1 : k;
    return gb + (size_t)((unsigned)(blk * X_BLK1 + k) * KSTR);
  };
  const unsigned aA = smb + 16384 + 16 * lane;
  unsigned aB[2][2];
#pragma unroll
  for (int t = 0; t < 2; ++t)
#pragma unroll
    for (int tt = 0; tt < 2; ++tt) {
      const int row = 8 * g + 4 * tt + qq;
      const int ch = (2 * (2 * tw + t) + (pp >> 1)) ^ (((row & 3) << 2) | ((row >> 2) & 3));
      aB[t][tt] = smb + row * 256 + 16 * ch + 8 * (pp & 1);
    }
  f32x4 acc[NA][2];
#pragma unroll
  for (int i = 0; i < NA; ++i) {
    acc[i][0] = (f32x4){0, 0, 0, 0}; acc[i][1] = (f32x4){0, 0, 0, 0};
    a_pin(acc[i][0]); a_pin(acc[i][1]);
  }
  float ssum = 0, ssq = 0;
  a_pin(ssum); a_pin(ssq);
  G1Set<UH> R0, R1;

  auto rd = [&](auto J, auto KS, G1Set<UH>& R, unsigned cA, const unsigned (&cB)[2][2]) {
    constexpr int j = J, ks = KS;
    if constexpr (j < 4) a_dstr<ks * 8192>(R.b[j >> 1][j & 1], cB[j >> 1][j & 1]);
    else { constexpr int a = j - 4; a_dsr128<(4 * (UB + (a >> 1)) + 2 * (a & 1) + ks) * 1024>(R.a[a], cA); }
  };
  auto mm = [&](auto M, G1Set<UH>& R) {
    constexpr int m = M, a = m >> 1, t = m & 1;
    const v4u bb = {R.b[t][0].x, R.b[t][0].y, R.b[t][1].x, R.b[t][1].y};
    a_mfma(acc[a][t], R.a[a], bb);
  };
  auto stat = [&](auto S, G1Set<UH>& R) {
    constexpr int s = S, e = s >> 1;
    const unsigned hw = e == 0 ? R.b[UH][0].x : e == 1 ? R.b[UH][0].y : e == 2 ? R.b[UH][1].x : R.b[UH][1].y;
    a_stat<s & 1>(ssum, ssq, hw);
  };
  auto block = [&](auto KS, auto RGc, auto CNT, auto DO_DMA, G1Set<UH>& X, G1Set<UH>& Y, unsigned cA, const unsigned (&cB)[2][2],
                   const unsigned char* base, unsigned m0v) {
    constexpr int RG = RGc;
    if constexpr (DO_DMA) a_m0(m0v);
    sfor<0, NM>([&](auto M) {
      constexpr int m = M;
      constexpr int issued = RG == 0 ? 0 : (m * NR + RG - 1) / RG < NR ? (m * NR + RG - 1) / RG : NR;
      if constexpr (CNT && (m & 1) == 0) a_waitl<(NR - 1 - (4 + m / 2)) + issued>();
      mm(M, X);
      if constexpr (RG > 0) {
        constexpr int nxt = ((m + 1) * NR + RG - 1) / RG < NR ? ((m + 1) * NR + RG - 1) / RG : NR;
        sfor<issued, nxt>([&](auto J) { rd(J, KS, Y, cA, cB); });
      }
      if constexpr (DO_DMA) {
        sfor<0, ND>([&](auto Dd) { if constexpr ((decltype(Dd)::value * NM + NM / 2) / ND == m) dma(Dd, base); });
      }
      sfor<0, 8>([&](auto S) { if constexpr ((decltype(S)::value * NM + NM / 2) / 8 == m) stat(S, X); });
    });
  };
  using T1 = std::true_type; using T0 = std::false_type;
  using K0 = std::integral_constant<int, 0>; using K1 = std::integral_constant<int, 1>;
#define X_RGB 4
  using RGA = std::integral_constant<int, NM>;
  using RGB = std::integral_constant<int, NM - X_RGB>;

  if (UH == 1 && tw == 0) {
    const unsigned co = lane < 28 ? 64 * u0 + 16 * lane : 4 * F + 64 * u0 + 16 * ((lane < 56 ? lane : 55) - 28);
    a_dma(smb + G1_CS, co, p.ws + WS_CS);
  }
  a_m0(m0s);
  { const unsigned char* b0 = kbase(0); asm volatile("s_nop 0"); sfor<0, ND>([&](auto I) { dma(I, b0); }); }
  a_m0(m0s + G1_STAGE);
  { const unsigned char* b1 = kbase(1); asm volatile("s_nop 0"); sfor<0, ND>([&](auto I) { dma(I, b1); }); }
  a_waitv<ND>();
  a_bar();
  SC(stp, 13);
  {
    unsigned cB[2][2] = {{aB[0][0], aB[0][1]}, {aB[1][0], aB[1][1]}};
    sfor<0, NR>([&](auto J) { rd(J, K0{}, R0, aA, cB); });
    a_m0(m0s + 2 * G1_STAGE);
    { const unsigned char* b2 = kbase(2); asm volatile("s_nop 0"); sfor<0, ND>([&](auto I) { dma(I, b2); }); }
    block(K1{}, RGB{}, T1{}, T0{}, R0, R1, aA, cB, nullptr, 0);
  }
  unsigned so = 0;
  auto step = [&](int it, auto DO_DMA, auto NW) {
    const unsigned sn = so;
    so += G1_STAGE; so = so == 3 * G1_STAGE ? 0 : so;
    const unsigned cA = aA + so;
    const unsigned cB[2][2] = {{aB[0][0] + so, aB[0][1] + so}, {aB[1][0] + so, aB[1][1] + so}};
    const unsigned char* base = kbase(it + 2);
    a_waitv<decltype(NW)::value>();
    a_lgkm0();
    a_bar();
    block(K0{}, RGA{}, T0{}, DO_DMA, R1, R0, cA, cB, base, m0s + sn);
    block(K1{}, RGB{}, T1{}, T0{}, R0, R1, cA, cB, nullptr, 0);
  };
  for (int it = 1; it < 8; ++it) step(it, T1{}, std::integral_constant<int, ND>{});
  step(8, T0{}, std::integral_constant<int, ND>{});
  step(9, T0{}, std::integral_constant<int, 0>{});
  a_lgkm0();
  {
    const unsigned cB[2][2] = {{0, 0}, {0, 0}};
    block(K0{}, std::integral_constant<int, 0>{}, T0{}, T0{}, R1, R0, 0, cB, nullptr, 0);
  }
  asm volatile("s_nop 15\n\ts_nop 3" ::: "memory");
  SC(stp, 14);
  LAS float* red = (LAS float*)(sm + G1_RED);
  {
    const float mean = xrow16(ssum) * (1.0f / D);
    const float var = fmaxf(xrow16(ssq) * (1.0f / D) - mean * mean, 0.f);
    const float sd = rbf(__builtin_amdgcn_sqrtf(rbf(rbf(var) + 1e-5f)));
    if (g == 0) *(LAS f32x2*)(red + 2 * (32 * tw + 16 * UH + r)) = (f32x2){rbf(mean), __builtin_amdgcn_rcpf(sd)};
  }
  BAR();
  f32x4 c1[NU], c3[NU];
#pragma unroll
  for (int i = 0; i < NU; ++i) {
    c1[i] = *(LAS f32x4*)(sm + G1_CS + 64 * (UB + i) + 16 * g);
    c3[i] = *(LAS f32x4*)(sm + G1_CS + 448 + 64 * (UB + i) + 16 * g);
  }
  f32x2 st[2];
#pragma unroll
  for (int t = 0; t < 2; ++t) st[t] = *(LAS f32x2*)(red + 2 * (32 * tw + 16 * t + r));
  a_lgkm0();
#pragma unroll
  for (int i = 0; i < NU; ++i) { a_pin(c1[i]); a_pin(c3[i]); }
  a_pin(st[0]); a_pin(st[1]);
  after_loop();
  SC(stp, 15);
#pragma unroll
  for (int i = 0; i < NU; ++i) {
    if (UB + i < nu) {
      const int un = u0 + UB + i;
      v2u o[2];
#pragma unroll
      for (int t = 0; t < 2; ++t) {
        const f32x2 nm = {-st[t].x, -st[t].x};
        const float ra1 = st[t].y * -1.4426950408889634f, r21 = st[t].y * st[t].y;
        const f32x2 ra = {ra1, ra1}, r2 = {r21, r21}, one = {1.f, 1.f};
        unsigned pk[2];
#pragma unroll
        for (int e = 0; e < 2; ++e) {
          const f32x2 cg2 = {c1[i][2 * e], c1[i][2 * e + 1]}, cu2 = {c3[i][2 * e], c3[i][2 * e + 1]};
          const f32x2 a = fma2(nm, cg2, (f32x2){acc[2 * i][t][2 * e], acc[2 * i][t][2 * e + 1]});
          const f32x2 bq = fma2(nm, cu2, (f32x2){acc[2 * i + 1][t][2 * e], acc[2 * i + 1][t][2 * e + 1]});
          const f32x2 arg = a * ra;
          const f32x2 den = one + (f32x2){__builtin_amdgcn_exp2f(arg.x), __builtin_amdgcn_exp2f(arg.y)};
          const f32x2 sg = {__builtin_amdgcn_rcpf(den.x), __builtin_amdgcn_rcpf(den.y)};
          const f32x2 hv = (a * bq) * (sg * r2);
          pk[e] = pkbf(hv.x, hv.y);
        }
        o[t] = (v2u){pk[0], pk[1]};
      }
      auto sx = __builtin_amdgcn_permlane16_swap(o[0].x, o[1].x, false, false);
      auto sy = __builtin_amdgcn_permlane16_swap(o[0].y, o[1].y, false, false);
      const v4u ov = {(unsigned)sx[0], (unsigned)sy[0], (unsigned)sx[1], (unsigned)sy[1]};
      const int ttile = 8 * rg + 2 * tw + (g & 1), chunk = 2 * (un & 1) + (g >> 1);
      ws_st128(p, (unsigned)WS_HM + (ttile * 54 + (un >> 1)) * 1024 + chunk * 256 + 16 * r, ov);
    }
  }
  SC(stp, 16);
}

#define X_BLK2 6
constexpr int G2_KS = 3;
constexpr int G2_NT = 54 / G2_KS;
constexpr int G2_STAGE = 9 * G2_KS * 1024;
constexpr int G2_NST = 5;
constexpr int G2_H = G2_NST * G2_STAGE;
constexpr int G2_LDS = G2_H + 80 * 128;

template <int DH> struct G2Set {
  v4u b[G2_KS];
  v4u a[DH ? 2 : 3][G2_KS];
};

template <int DH, int NDMA>
DI void g2_dma_tile(const Params& p, unsigned smb, int lane, int tt, int t0, int dg, int kt0, int it, unsigned so) {
  constexpr int KS = G2_KS;
  const unsigned char* gb = DH ? p.ws + WS_W2 + ((size_t)dg * 18 * 15 + 4 * tt) * 1024 : p.ws + WS_HM + (size_t)((t0 >> 4) + tt) * 54 * 1024;
  constexpr unsigned KSTR = DH ? 15 * 1024 : KS * 1024;
  const int blk = it / X_BLK2;
  int k = it - blk * X_BLK2 + kt0; k = k >= X_BLK2 ? k - X_BLK2 : k;
  const unsigned char* base = gb + (size_t)((unsigned)(blk * X_BLK2 + k) * KSTR);
  a_m0n(smb + (DH ? 4 * KS + 4 * tt : KS * tt) * 1024 + so);
  sfor<0, NDMA>([&](auto I) { a_dma_o<1024 * decltype(I)::value>(16 * lane, base); });
}
template <int DH, int NDMA>
DI void g2_prefetch(const Params& p, LAS char* sm, int lane, int tt, int t0, int d0, int dg, int kt0) {
  const unsigned smb = lds_addr(sm);
  if constexpr (DH == 0) {
    const unsigned char* hb = p.ws + WS_HT + ((size_t)d0 * L + t0) * 2;
    const unsigned ho = (unsigned)((lane >> 3) * L * 2 + (lane & 7) * 16);
    a_dma(smb + G2_H + tt * 1024, ho, hb + (size_t)tt * 8 * L * 2);
    a_dma(smb + G2_H + (tt + 4) * 1024, ho, hb + (size_t)(tt + 4) * 8 * L * 2);
    if (tt < 2) a_dma(smb + G2_H + (tt + 8) * 1024, ho, hb + (size_t)(tt + 8) * 8 * L * 2);
  } else {
#pragma unroll
    for (int i = 0; i < G2_NST - 1; ++i) g2_dma_tile<1, NDMA>(p, smb, lane, tt, t0, dg, kt0, i, i * G2_STAGE);
  }
}

template <int DH, int NDMA>
DI void g2_wave(const Params& p, LAS char* sm, int lane, int tt, int t0, int d0, int dg, int kt0, int rg, unsigned tag, Stamps& stp) {
  constexpr int ND = DH ? 2 : 3, DB = DH ? 3 : 0, NR = G2_KS * (1 + ND), NM = G2_KS * ND, KS = G2_KS;
  const unsigned smb = lds_addr(sm);
  auto dma_tile = [&](int it, unsigned so) { g2_dma_tile<DH, NDMA>(p, smb, lane, tt, t0, dg, kt0, it, so); };
  const unsigned aB = smb + KS * tt * 1024 + 16 * lane, aA = smb + (4 * KS + DB * KS) * 1024 + 16 * lane;
  f32x4 acc[ND];
#pragma unroll
  for (int i = 0; i < ND; ++i) { acc[i] = (f32x4){0, 0, 0, 0}; a_pin(acc[i]); }
  G2Set<DH> R0, R1;
  auto rd = [&](auto J, G2Set<DH>& R, unsigned so) {
    constexpr int j = J;
    if constexpr (j < KS) a_dsr128<j * 1024>(R.b[j], aB + so);
    else { constexpr int a = (j - KS) / KS, ks = (j - KS) % KS; a_dsr128<(a * KS + ks) * 1024>(R.a[a][ks], aA + so); }
  };
  auto mm = [&](auto M, G2Set<DH>& R) {
    constexpr int m = M, ks = m / ND, a = m % ND;
    a_mfma(acc[a], R.a[a][ks], R.b[ks]);
  };
  group_wait(p, rg, lane, DH == 0 ? tt : 4, tag);
  SC(stp, 18);
  if constexpr (DH == 0) {
#pragma unroll
    for (int i = 0; i < G2_NST - 1; ++i) dma_tile(i, i * G2_STAGE);
  }
  auto waitv = [&](auto N) { a_waitv<decltype(N)::value>(); };
  auto step = [&](int it, unsigned so, unsigned sn, auto FIRST, auto DODMA, G2Set<DH>& X, G2Set<DH>& Y) {
    a_lgkm0();
    a_bar();
    if constexpr (FIRST) {
      sfor<0, NR>([&](auto J) { rd(J, Y, so); });
      if constexpr (DODMA) dma_tile(it + G2_NST - 1, sn);
    } else {
      sfor<0, NM>([&](auto M) {
        constexpr int m = M;
        mm(M, X);
        if constexpr (2 * m < NR) rd(std::integral_constant<int, 2 * m>{}, Y, so);
        if constexpr (2 * m + 1 < NR) rd(std::integral_constant<int, 2 * m + 1>{}, Y, so);
        if constexpr (m == NM - 1) {
          sfor<(2 * NM < NR ? 2 * NM : NR), NR>([&](auto J) { rd(J, Y, so); });
          if constexpr (DODMA) dma_tile(it + G2_NST - 1, sn);
        }
      });
    }
  };
  using T1 = std::true_type; using T0 = std::false_type;
  constexpr int INF = (G2_NST - 2) * NDMA;
  waitv(std::integral_constant<int, INF>{});
  SC(stp, 19);
  step(0, 0, (G2_NST - 1) * G2_STAGE, T1{}, T1{}, R1, R0);
  unsigned so = 0;
  auto nxt = [&](unsigned s) { s += G2_STAGE; return s == G2_NST * G2_STAGE ? 0u : s; };
  auto prv = [&](unsigned s) { return s == 0 ? (unsigned)((G2_NST - 1) * G2_STAGE) : s - G2_STAGE; };
  constexpr int NSTEADY = G2_NT - G2_NST;
  for (int it = 1; it + 1 <= NSTEADY; it += 2) {
    so = nxt(so);
    waitv(std::integral_constant<int, INF>{});
    step(it, so, prv(so), T0{}, T1{}, R0, R1);
    so = nxt(so);
    waitv(std::integral_constant<int, INF>{});
    step(it + 1, so, prv(so), T0{}, T1{}, R1, R0);
  }
  static_assert(NSTEADY == 13 && G2_NST == 5, "tail below is written for this shape");
  so = nxt(so); waitv(std::integral_constant<int, INF>{});       step(13, so, prv(so), T0{}, T1{}, R0, R1);
  so = nxt(so); waitv(std::integral_constant<int, INF>{});       step(14, so, 0, T0{}, T0{}, R1, R0);
  so = nxt(so); waitv(std::integral_constant<int, 2 * NDMA>{});  step(15, so, 0, T0{}, T0{}, R0, R1);
  so = nxt(so); waitv(std::integral_constant<int, NDMA>{});      step(16, so, 0, T0{}, T0{}, R1, R0);
  so = nxt(so); waitv(std::integral_constant<int, 0>{});         step(17, so, 0, T0{}, T0{}, R0, R1);
  a_lgkm0();
  sfor<0, NM>([&](auto M) { mm(M, R1); });
  asm volatile("s_nop 15\n\ts_nop 3" ::: "memory");
  SC(stp, 20);
  const int r = lane & 15, g = lane >> 4, qq = (lane & 15) >> 2, pp = lane & 3;
  v2u hv[ND];
#pragma unroll
  for (int i = 0; i < ND; ++i) a_dstr<0>(hv[i], smb + G2_H + (16 * (DB + i) + 4 * g + qq) * 128 + 32 * tt + 8 * pp);
  a_lgkm0();
#pragma unroll
  for (int i = 0; i < ND; ++i) a_pin(hv[i]);
#pragma unroll
  for (int i = 0; i < ND; ++i) {
    const float o0 = bflo(hv[i].x) + rbf(acc[i][0]), o1 = bfhi(hv[i].x) + rbf(acc[i][1]);
    const float o2 = bflo(hv[i].y) + rbf(acc[i][2]), o3 = bfhi(hv[i].y) + rbf(acc[i][3]);
    *(GAS v2u*)(p.out + (size_t)(t0 + 16 * tt + r) * D + d0 + 16 * (DB + i) + 4 * g) = (v2u){pkbf(o0, o1), pkbf(o2, o3)};
  }
  SC(stp, 21);
}

constexpr int K_LDS = A_LDS > G1_LDS ? (A_LDS > G2_LDS ? A_LDS : G2_LDS) : (G1_LDS > G2_LDS ? G1_LDS : G2_LDS);
static_assert(K_LDS <= 163840 && K_LDS > 81920, "one workgroup per CU");

__global__ __launch_bounds__(512, 2) void k_block(Params p) {
  extern __shared__ __attribute__((aligned(16))) char smem[];
  LAS char* sm = (LAS char*)smem;
  const int tid = threadIdx.x, lane = tid & 63, w = __builtin_amdgcn_readfirstlane(tid >> 6);
  const int b = blockIdx.x, xl = b & 7, j = b >> 3;
  Stamps stp;
  SR(stp, 30);
  SC(stp, 0);
  unsigned tag;
  if (j < 20) {
    tag = conv_role(p, sm, tid, lane, w, 20 * xl + j, stp);
  } else {
    const int hidx = (j - 20) * 8 + xl;
    tag = ws_ld(p, WS_GEN) + 1u;
    if (hidx < 64) ln_rows(p, 4 * (8 * hidx + w), lane, tag);
    if (hidx == 95 && w == 7) {
      ws_st(p, WS_CNT + lane * 128, 0u);
      ws_st(p, WS_CNT + (64 + lane) * 128, 0u);
      WAITV(0);
      if (lane == 0) ws_st(p, WS_RDY, tag);
    }
    helper_role(p, sm, lane, w, hidx);
    SC(stp, 9);
  }
  tag = __builtin_amdgcn_readfirstlane(tag);
  sync_all(p, b, lane, w, tag);
  SC(stp, 12);
  const int rgi = j & 7, cgi = j >> 3;
  const int rg = 8 * (xl & 1) + rgi, cg = 4 * (xl >> 1) + cgi;
  const int th = (cg >> 2) & 1, dg = 4 * (cg >> 3) + (cg & 3);
  const int t0 = 128 * rg + 64 * th, d0 = 80 * dg;
  const int kt2 = (rgi + cgi) % X_BLK2;
  {
    const int u0 = cg < 12 ? 7 * cg : 84 + 6 * (cg - 12), nu = cg < 12 ? 7 : 6;
    const int kt0 = (rgi + cgi) % X_BLK1;
    if (w < 4) g1_wave<0>(p, sm, lane, w, rg, cg, u0, nu, kt0, stp, [&] { g2_prefetch<0, 3>(p, sm, lane, w, t0, d0, dg, kt2); });
    else if (w < 7) g1_wave<1>(p, sm, lane, w - 4, rg, cg, u0, nu, kt0, stp, [&] { g2_prefetch<1, 4>(p, sm, lane, w - 4, t0, d0, dg, kt2); });
    else g1_wave<1>(p, sm, lane, 3, rg, cg, u0, nu, kt0, stp, [&] { g2_prefetch<1, 3>(p, sm, lane, 3, t0, d0, dg, kt2); });
  }
  group_arrive(p, rg, cg, tid, tag);
  SC(stp, 17);
  if (w < 4) g2_wave<0, 3>(p, sm, lane, w, t0, d0, dg, kt2, rg, tag, stp);
  else if (w < 7) g2_wave<1, 4>(p, sm, lane, w - 4, t0, d0, dg, kt2, rg, tag, stp);
  else g2_wave<1, 3>(p, sm, lane, 3, t0, d0, dg, kt2, rg, tag, stp);
  SR(stp, 31);
  SCW(stp);
}

extern "C" void kernel_launch(void* const* d_in, const int* in_sizes, int n_in, void* d_out, int out_size, void* d_ws,
                              size_t ws_size, hipStream_t stream) {
  Params p;
  p.x = (const u16*)d_in[0];
  p.logA = (const u16*)d_in[1];
  p.Aim = (const u16*)d_in[2];
  p.Cre = (const u16*)d_in[3];
  p.Cim = (const u16*)d_in[4];
  p.logdt = (const u16*)d_in[5];
  p.Dsk = (const u16*)d_in[6];
  p.w1 = (const u16*)d_in[8];
  p.w3 = (const u16*)d_in[9];
  p.w2 = (const u16*)d_in[10];
  p.out = (u16*)d_out;
  p.ws = (unsigned char*)d_ws;
  (void)hipFuncSetAttribute((const void*)k_block, hipFuncAttributeMaxDynamicSharedMemorySize, K_LDS);
  k_block<<<256, 512, K_LDS, stream>>>(p);
}
```

```cpp
#include <hip/hip_runtime.h>
#include <hip/hip_bf16.h>
#include <stdint.h>
#include <type_traits>

#define GAS __attribute__((address_space(1)))
#define LAS __attribute__((address_space(3)))
#define DI __device__ __forceinline__

typedef unsigned short u16;
typedef __bf16 bf16x8 __attribute__((ext_vector_type(8)));
typedef __bf16 bf16x2 __attribute__((ext_vector_type(2)));
typedef _Float16 f16x8 __attribute__((ext_vector_type(8)));
typedef _Float16 f16x2 __attribute__((ext_vector_type(2)));
typedef short s16x4 __attribute__((ext_vector_type(4)));
typedef float f32x4 __attribute__((ext_vector_type(4)));
typedef float f32x2 __attribute__((ext_vector_type(2)));
typedef unsigned v4u __attribute__((ext_vector_type(4)));
typedef unsigned v2u __attribute__((ext_vector_type(2)));
typedef GAS unsigned gu32;
typedef GAS unsigned long long gu64;

constexpr int L = 2048, D = 640, NM = 128, F = 1728;

constexpr size_t WS_GEN = 0;
constexpr size_t WS_RDY = 128;
constexpr size_t WS_FLG = 1024;
constexpr size_t WS_CNT = 4096;
constexpr size_t WS_STATS = 20480;
constexpr size_t WS_CS = 36864;
constexpr size_t WS_HT = 65536;
constexpr size_t WS_W13 = WS_HT + (size_t)D * L * 2;
constexpr size_t WS_W2 = WS_W13 + (size_t)16 * 10 * 28 * 1024;
constexpr size_t WS_HM = WS_W2 + (size_t)8 * 18 * 15 * 1024;
constexpr size_t WS_END = WS_HM + (size_t)128 * 54 * 1024;

struct Params {
  const u16* x;
  const u16* logA;
  const u16* Aim;
  const u16* Cre;
  const u16* Cim;
  const u16* logdt;
  const u16* Dsk;
  const u16* w1;
  const u16* w3;
  const u16* w2;
  u16* out;
  unsigned char* ws;
};

DI float bf2f(unsigned b) { return __builtin_bit_cast(float, b << 16); }
DI float bflo(unsigned v) { return __builtin_bit_cast(float, v << 16); }
DI float bfhi(unsigned v) { return __builtin_bit_cast(float, v & 0xffff0000u); }
DI unsigned pkbf(float a, float b) {
  bf16x2 r = {(__bf16)a, (__bf16)b};
  return __builtin_bit_cast(unsigned, r);
}
DI unsigned pkh(float a, float b) {
  f16x2 r = {(_Float16)a, (_Float16)b};
  return __builtin_bit_cast(unsigned, r);
}
DI float rbf(float a) { return bflo(pkbf(a, a)); }
DI float hlo(unsigned v) { return (float)__builtin_bit_cast(f16x2, v)[0]; }
DI float hhi(unsigned v) { return (float)__builtin_bit_cast(f16x2, v)[1]; }

template <int CTRL> DI float dppf(float v) {
  return __builtin_bit_cast(float, __builtin_amdgcn_update_dpp(0, __builtin_bit_cast(int, v), CTRL, 0xf, 0xf, true));
}
DI float xrow16(float v) {
  unsigned u = __builtin_bit_cast(unsigned, v);
  auto a = __builtin_amdgcn_permlane16_swap(u, u, false, false);
  v = __builtin_bit_cast(float, (unsigned)a[0]) + __builtin_bit_cast(float, (unsigned)a[1]);
  u = __builtin_bit_cast(unsigned, v);
  auto b = __builtin_amdgcn_permlane32_swap(u, u, false, false);
  return __builtin_bit_cast(float, (unsigned)b[0]) + __builtin_bit_cast(float, (unsigned)b[1]);
}
DI float wave_sum(float v) {
  v += dppf<0x128>(v);
  v += dppf<0x124>(v);
  v += dppf<0x122>(v);
  v += dppf<0x121>(v);
  return xrow16(v);
}
template <int CTRL> DI unsigned dppu(unsigned v) { return (unsigned)__builtin_amdgcn_update_dpp(0, (int)v, CTRL, 0xf, 0xf, true); }
DI float wave_max_abs(unsigned u) {
  u = max(u, dppu<0x128>(u));
  u = max(u, dppu<0x124>(u));
  u = max(u, dppu<0x122>(u));
  u = max(u, dppu<0x121>(u));
  auto a = __builtin_amdgcn_permlane16_swap(u, u, false, false);
  u = max((unsigned)a[0], (unsigned)a[1]);
  auto b = __builtin_amdgcn_permlane32_swap(u, u, false, false);
  return __builtin_bit_cast(float, max((unsigned)b[0], (unsigned)b[1]));
}
DI f32x4 mfma_bf(v4u a, v4u b, f32x4 c) {
  return __builtin_amdgcn_mfma_f32_16x16x32_bf16(__builtin_bit_cast(bf16x8, a), __builtin_bit_cast(bf16x8, b), c, 0, 0, 0);
}
DI f32x4 mfma_h(v4u a, v4u b, f32x4 c) {
  return __builtin_amdgcn_mfma_f32_16x16x32_f16(__builtin_bit_cast(f16x8, a), __builtin_bit_cast(f16x8, b), c, 0, 0, 0);
}
DI v2u trread(LAS char* p) {
  s16x4 v = __builtin_amdgcn_ds_read_tr16_b64_v4i16((LAS s16x4*)p);
  return __builtin_bit_cast(v2u, v);
}
DI v4u ldsr128(LAS char* p) { return *(LAS v4u*)p; }
DI v2u ldsr64(LAS char* p) { return *(LAS v2u*)p; }
DI void glds16(const void* g, LAS char* l) {
  __builtin_amdgcn_global_load_lds((const GAS void*)g, (LAS void*)l, 16, 0, 0);
}
#define RLX_AGENT __ATOMIC_RELAXED, __HIP_MEMORY_SCOPE_AGENT
#define WAITV(n) asm volatile("s_waitcnt vmcnt(" #n ")" ::: "memory")
#define BAR() do { asm volatile("s_waitcnt lgkmcnt(0)" ::: "memory"); __builtin_amdgcn_s_barrier(); asm volatile("" ::: "memory"); } while (0)

struct Stamps {};
#define SC(st, i)
#define SR(st, i)
#define SCW(st)
DI f32x2 cmul(f32x2 a, f32x2 b) {
  f32x2 t, r;
  asm("v_pk_mul_f32 %0, %1, %2 op_sel_hi:[1,0]" : "=v"(t) : "v"(a), "v"(b));
  asm("v_pk_fma_f32 %0, %1, %2, %3 op_sel:[1,1,0] op_sel_hi:[0,1,1] neg_lo:[0,1,0]" : "=v"(r) : "v"(a), "v"(b), "v"(t));
  return r;
}
DI f32x2 unbf(unsigned v) { return (f32x2){bflo(v), bfhi(v)}; }
DI f32x2 fma2(f32x2 a, f32x2 b, f32x2 c) { return __builtin_elementwise_fma(a, b, c); }

template <int I, int N, class Fn> DI void sfor(Fn&& f) {
  if constexpr (I < N) { f(std::integral_constant<int, I>{}); sfor<I + 1, N>(f); }
}
DI void a_mfma(f32x4& c, const v4u& a, const v4u& b) {
  asm volatile("v_mfma_f32_16x16x32_bf16 %0, %1, %2, %0" : "+v"(c) : "v"(a), "v"(b));
}
DI void a_mfma_h(f32x4& c, const v4u& a, const v4u& b) {
  asm volatile("v_mfma_f32_16x16x32_f16 %0, %1, %2, %0" : "+v"(c) : "v"(a), "v"(b));
}
template <int OFF> DI void a_dsr64(v2u& d, unsigned addr) {
  asm volatile("ds_read_b64 %0, %1 offset:%2" : "=v"(d) : "v"(addr), "n"(OFF));
}
template <int OFF> DI void a_dsr128(v4u& d, unsigned addr) {
  asm volatile("ds_read_b128 %0, %1 offset:%2" : "=v"(d) : "v"(addr), "n"(OFF));
}
template <int OFF> DI void a_dsr16(unsigned& d, unsigned addr) {
  asm volatile("ds_read_u16 %0, %1 offset:%2" : "=v"(d) : "v"(addr), "n"(OFF));
}
template <int OFF> DI void a_dsw128(unsigned addr, const v4u& v) {
  asm volatile("ds_write_b128 %0, %1 offset:%2" ::"v"(addr), "v"(v), "n"(OFF) : "memory");
}
template <int OFF> DI void a_dstr(v2u& d, unsigned addr) {
  asm volatile("ds_read_b64_tr_b16 %0, %1 offset:%2" : "=v"(d) : "v"(addr), "n"(OFF));
}
DI void a_dma(unsigned lds, unsigned voff, const void* gbase) {
  asm volatile("s_mov_b32 m0, %0\n\ts_nop 0\n\tglobal_load_lds_dwordx4 %1, %2 sc1" ::"s"(lds), "v"(voff), "s"(gbase) : "memory");
}
template <int OFF> DI void a_ds_tid(unsigned v) { asm volatile("ds_write_addtid_b32 %0 offset:%1" ::"v"(v), "n"(OFF) : "memory"); }
DI void a_m0(unsigned lds) { asm volatile("s_mov_b32 m0, %0" ::"s"(lds) : "memory"); }
DI void a_m0n(unsigned lds) { asm volatile("s_mov_b32 m0, %0\n\ts_nop 0" ::"s"(lds) : "memory"); }
template <int OFF> DI void a_dma_o(unsigned voff, const void* gbase) {
  asm volatile("global_load_lds_dwordx4 %0, %1 offset:%2 sc1" ::"v"(voff), "s"(gbase), "n"(OFF) : "memory");
}
DI void a_dot_sq(float& c, unsigned v) { asm volatile("v_dot2c_f32_bf16 %0, %1, %1" : "+v"(c) : "v"(v)); }
DI void a_dot_one(float& c, unsigned v) { asm volatile("v_dot2c_f32_bf16 %0, 0x3f803f80, %1" : "+v"(c) : "v"(v)); }
template <int HI> DI void a_stat(float& sum, float& sq, unsigned v) {
  float x;
  if constexpr (HI)
    asm volatile("v_and_b32 %2, 0xffff0000, %3\n\tv_add_f32 %0, %0, %2\n\tv_fmac_f32 %1, %2, %2" : "+v"(sum), "+v"(sq), "=&v"(x) : "v"(v));
  else
    asm volatile("v_lshlrev_b32 %2, 16, %3\n\tv_add_f32 %0, %0, %2\n\tv_fmac_f32 %1, %2, %2" : "+v"(sum), "+v"(sq), "=&v"(x) : "v"(v));
}
template <int N> DI void a_waitl() { static_assert(N >= 0 && N <= 15, "lgkmcnt"); asm volatile("s_waitcnt lgkmcnt(%0)" ::"n"(N) : "memory"); }
template <int N> DI void a_waitv() { asm volatile("s_waitcnt vmcnt(%0)" ::"n"(N) : "memory"); }
template <class T> DI void a_pin(T& v) { asm volatile("" : "+v"(v)); }
DI void a_lgkm0() { asm volatile("s_waitcnt lgkmcnt(0)" ::: "memory"); }
DI void a_bar() { asm volatile("s_barrier" ::: "memory"); }
DI unsigned lds_addr(LAS char* p) { return (unsigned)(size_t)p; }

DI void ws_st128(const Params& p, unsigned off, v4u v) {
  __builtin_amdgcn_raw_buffer_store_b128(v, __builtin_amdgcn_make_buffer_rsrc(p.ws, 0, (unsigned)WS_END, 0x00020000), off, 0, 16);
}
constexpr unsigned SPIN_CAP = 1u << 18;
DI unsigned ws_ld(const Params& p, size_t off) { return __hip_atomic_load((gu32*)(p.ws + off), RLX_AGENT); }
DI void ws_st(const Params& p, size_t off, unsigned v) { __hip_atomic_store((gu32*)(p.ws + off), v, RLX_AGENT); }
DI void sync_all(const Params& p, int b, int lane, int w, unsigned tag, Stamps& stp) {
  unsigned rdy = 0;
  if (w == 0) rdy = ws_ld(p, WS_RDY);
  WAITV(0);
  SC(stp, 22);
  a_bar();
  SC(stp, 23);
  if (w == 0) {
    for (unsigned spin = 0; rdy != tag && spin < SPIN_CAP; ++spin) rdy = ws_ld(p, WS_RDY);
    if (lane < 8) __hip_atomic_fetch_add((gu32*)(p.ws + WS_CNT + (lane * 16 + (b >> 4)) * 128), 1u, RLX_AGENT);
    asm volatile("" ::: "memory");
    SC(stp, 24);
    const size_t mine = WS_CNT + ((b & 7) * 16 + (lane & 15)) * 128;
    for (unsigned spin = 0; spin < SPIN_CAP; ++spin)
      if (__all(ws_ld(p, mine) == 16u)) break;
    if (b == 0 && lane == 0) ws_st(p, WS_GEN, tag);
  }
  a_bar();
}
DI void group_arrive(const Params& p, int rg, int cg, int tid, unsigned tag) {
  WAITV(0);
  a_bar();
  if (tid == 0) ws_st(p, WS_FLG + rg * 128 + cg * 4, tag);
}
DI void group_wait(const Params& p, int rg, int lane, int w, unsigned tag) {
  if (w == 0) {
    for (unsigned spin = 0; spin < SPIN_CAP; ++spin)
      if (__all(ws_ld(p, WS_FLG + rg * 128 + (lane & 15) * 4) == tag)) break;
  }
  a_bar();
}

constexpr int T_CH = 1344;
constexpr int A_T = 0;
constexpr int A_SC = 4 * T_CH;
constexpr int KG_ROW = 272;
constexpr int KG_GA = 21 * KG_ROW, KG_EB = 32 * KG_ROW;
constexpr int A_KG = A_SC + 64;
constexpr int A_GA = A_KG, A_EB = A_GA + 8 * KG_GA;
static_assert(7 * KG_EB < 65536 && A_EB + 31 * KG_ROW < 65536, "ds_write_addtid: M0 and offset are 16 bits each");
constexpr int KC_COPY = 4416, KC_CH = 4 * KC_COPY;
constexpr int A_EX = A_KG + 4 * KC_CH;
constexpr int A_U = A_EB + 8 * KG_EB;
constexpr int U_CH = 4608;
constexpr int A_X = A_U + 4 * U_CH;
constexpr int A_PX = A_X;
constexpr int A_LDS = A_X + 4 * 4096;
static_assert(A_EX + 8 * 4096 <= A_U, "lds map");
static_assert(A_LDS <= 163840 && A_KG % 64 == 0, "lds");

struct ExpTab {
  unsigned v[256 * 12];
  constexpr ExpTab() : v() {
    for (int c = 0; c < 256; ++c)
      for (int i = 0; i < 12; ++i) {
        const int l = 2051 - 8 * c - (i < 11 ? i : 10);
        int sh = 0;
        while ((l >> sh) >= 256) ++sh;
        int idx = l;
        if (sh > 0) idx = 128 * sh + ((l + (1 << (sh - 1)) - 1 + ((l >> sh) & 1)) >> sh);
        v[c * 12 + i] = 2u * (unsigned)idx;
      }
  }
};
__device__ const ExpTab g_exp_tab = ExpTab();

template <int OFF> DI void g_ld32(unsigned& d, unsigned voff, const void* base) {
  asm volatile("global_load_dword %0, %1, %2 offset:%3" : "=v"(d) : "v"(voff), "s"(base), "n"(OFF) : "memory");
}
DI void g_ld32_sc1(unsigned& d, unsigned voff, const void* base) {
  asm volatile("global_load_dword %0, %1, %2 sc1" : "=v"(d) : "v"(voff), "s"(base) : "memory");
}
DI void g_ld16(unsigned& d, unsigned voff, const void* base) {
  asm volatile("global_load_ushort %0, %1, %2" : "=v"(d) : "v"(voff), "s"(base) : "memory");
}
template <int OFF> DI void g_ld128(v4u& d, unsigned voff, const void* base) {
  asm volatile("global_load_dwordx4 %0, %1, %2 offset:%3" : "=v"(d) : "v"(voff), "s"(base), "n"(OFF) : "memory");
}
template <int OFF> DI void g_ld128_sc1(v4u& d, unsigned voff, const void* base) {
  asm volatile("global_load_dwordx4 %0, %1, %2 offset:%3 sc1" : "=v"(d) : "v"(voff), "s"(base), "n"(OFF) : "memory");
}
template <int OFF> DI void g_ld64(v2u& d, unsigned voff, const void* base) {
  asm volatile("global_load_dwordx2 %0, %1, %2 offset:%3" : "=v"(d) : "v"(voff), "s"(base), "n"(OFF) : "memory");
}

DI void ln_rows(const Params& p, int row0, int lane, unsigned tag) {
  const int row = row0 + (lane >> 4);
  v4u xr[5];
#pragma unroll
  for (int i = 0; i < 5; ++i) xr[i] = *(const GAS v4u*)(p.x + (size_t)row * D + 8 * ((lane & 15) + 16 * i));
  f32x2 s2[2] = {{0, 0}, {0, 0}}, q2[2] = {{0, 0}, {0, 0}};
#pragma unroll
  for (int i = 0; i < 5; ++i)
#pragma unroll
    for (int e = 0; e < 4; ++e) {
      const unsigned d = xr[i][e];
      const f32x2 v = unbf(d);
      s2[e & 1] += v;
      q2[e & 1] = fma2(v, v, q2[e & 1]);
    }
  float sm_ = (s2[0].x + s2[0].y) + (s2[1].x + s2[1].y), sq = (q2[0].x + q2[0].y) + (q2[1].x + q2[1].y);
  sm_ += dppf<0x128>(sm_); sq += dppf<0x128>(sq);
  sm_ += dppf<0x124>(sm_); sq += dppf<0x124>(sq);
  sm_ += dppf<0x122>(sm_); sq += dppf<0x122>(sq);
  sm_ += dppf<0x121>(sm_); sq += dppf<0x121>(sq);
  const float mean = sm_ * (1.0f / D);
  const float var = fmaxf(sq * (1.0f / D) - mean * mean, 0.f);
  const float sd = rbf(__builtin_amdgcn_sqrtf(rbf(rbf(var) + 1e-5f)));
  if ((lane & 15) == 0)
    __hip_atomic_store((gu64*)(p.ws + WS_STATS) + row, ((unsigned long long)tag << 32) | pkbf(mean, sd), RLX_AGENT);
}

struct ConvSet { v2u al[8], ah[8]; v4u b[8]; };

DI unsigned conv_role(const Params& p, LAS char* sm, int tid, int lane, int w, int cgp, Stamps& stp) {
  const int c0 = 4 * cgp;
  const unsigned smb = lds_addr(sm);
  const int kch = c0 + (w >> 1), hf = w & 1, n = 64 * hf + lane;
  const int ec = tid & 255, epair = w >> 2;
  unsigned gen, vdt, vla, vai, vcr, vci, vcr2, vci2, vdsk;
  v4u et[3];
  v2u xg[4];
  const unsigned go = (unsigned)(4 * tid) * (D * 2);
  const u16* xc = p.x + c0;
  {
    const unsigned zero = 0, po = 2 * (kch * NM + n), po2 = 2 * (kch * NM + (n ^ 64));
    g_ld16(vdt, zero, p.logdt + kch);
    g_ld16(vla, po, p.logA); g_ld16(vai, po, p.Aim); g_ld16(vcr, po, p.Cre); g_ld16(vci, po, p.Cim);
    g_ld16(vcr2, po2, p.Cre); g_ld16(vci2, po2, p.Cim);
    g_ld32_sc1(gen, zero, p.ws + WS_GEN);
    g_ld16(vdsk, zero, p.Dsk + c0 + (w & 3));
    g_ld128<0>(et[0], 48 * ec, g_exp_tab.v); g_ld128<16>(et[1], 48 * ec, g_exp_tab.v); g_ld128<32>(et[2], 48 * ec, g_exp_tab.v);
    asm volatile("s_barrier" ::: "memory");
    g_ld64<0>(xg[0], go, xc); g_ld64<1280>(xg[1], go, xc);
  }
  SC(stp, 10);
  asm volatile("s_waitcnt vmcnt(2)"
               : "+v"(gen), "+v"(vdt), "+v"(vla), "+v"(vai), "+v"(vcr), "+v"(vci), "+v"(vcr2), "+v"(vci2), "+v"(vdsk), "+v"(et[0]),
                 "+v"(et[1]), "+v"(et[2])::"memory");
  const unsigned tag = gen + 1u;
  SC(stp, 1);
  float kscale;
  {
    const float dt = rbf(__builtin_amdgcn_exp2f(bf2f(vdt) * 1.4426950408889634f));
    const float are = -rbf(__builtin_amdgcn_exp2f(bf2f(vla) * 1.4426950408889634f)) * dt, aim = bf2f(vai) * dt;
    const float cmax = wave_max_abs(max(max((vcr & 0x7fffu) << 16, (vci & 0x7fffu) << 16), max((vcr2 & 0x7fffu) << 16, (vci2 & 0x7fffu) << 16)));
    const float cinv = cmax > 0.f ? __builtin_amdgcn_rcpf(cmax) : 0.f;
    kscale = 2.0f * dt * cmax;
    const float al = are * 1.4426950408889634f, ar = aim * 0.15915494309189535f;
    auto cpow = [&](float pw) -> f32x2 {
      const float e = __builtin_amdgcn_exp2f(al * pw);
      const float f = __builtin_amdgcn_fractf(ar * pw);
      return (f32x2){e * __builtin_amdgcn_cosf(f), e * __builtin_amdgcn_sinf(f)};
    };
    const f32x2 w1 = cpow(1.f), w8 = cpow(8.f), w16 = cpow(16.f), w32 = cpow(32.f), W1 = cpow(256.f);
    f32x2 e[4] = {{1.f, 0.f}, w8, w16, cmul(w8, w16)};
    a_m0n(smb + w * KG_EB);
    sfor<0, 8>([&](auto J) {
      constexpr int j = 4 * decltype(J)::value;
      unsigned pk[4];
#pragma unroll
      for (int i = 0; i < 4; ++i) pk[i] = pkh(e[i].x, e[i].y);
      a_ds_tid<A_EB + (j + 0) * KG_ROW>(pk[0]); a_ds_tid<A_EB + (j + 1) * KG_ROW>(pk[1]);
      a_ds_tid<A_EB + (j + 2) * KG_ROW>(pk[2]); a_ds_tid<A_EB + (j + 3) * KG_ROW>(pk[3]);
      if constexpr (j + 4 < 32) {
#pragma unroll
        for (int i = 0; i < 4; ++i) e[i] = cmul(e[i], w32);
      }
    });
    g_ld64<2560>(xg[2], go, xc);
    const f32x2 v1 = {w1.x, -w1.y}, V1 = {W1.x, -W1.y};
    const f32x2 v2 = cmul(v1, v1), v4 = cmul(v2, v2);
    f32x2 g[21];
    g[0] = (f32x2){bf2f(vcr) * cinv, -bf2f(vci) * cinv};
    g[1] = cmul(g[0], v1); g[2] = cmul(g[0], v2); g[3] = cmul(g[1], v2);
#pragma unroll
    for (int r = 4; r < 8; ++r) g[r] = cmul(g[r - 4], v4);
    const f32x2 V2 = cmul(V1, V1), V3 = cmul(V2, V1), V4 = cmul(V2, V2);
#pragma unroll
    for (int bb = 0; bb < 4; ++bb) g[8 + bb] = cmul(g[2 * bb], V1);
    g[12] = cmul(g[0], V2); g[13] = cmul(g[4], V2); g[14] = cmul(g[0], V3); g[15] = cmul(g[4], V3);
    g[16] = cmul(g[0], V4);
    g[17] = cmul(g[16], V1); g[18] = cmul(g[16], V2); g[19] = cmul(g[16], V3); g[20] = cmul(g[16], V4);
    a_m0n(smb + w * KG_GA);
    sfor<0, 21>([&](auto R) { constexpr int rw = R; a_ds_tid<A_GA + rw * KG_ROW>(pkh(g[rw].x, g[rw].y)); });
    g_ld64<3840>(xg[3], go, xc);
  }
  f32x4 d0, d1;
  {
    const int r = lane & 15, q = lane >> 4;
    f32x4 d[2][2];
#pragma unroll
    for (int i = 0; i < 4; ++i) { d[i >> 1][i & 1] = (f32x4){0, 0, 0, 0}; a_pin(d[i >> 1][i & 1]); }
    a_lgkm0();
    const unsigned ga = smb + A_GA + w * KG_GA + r * KG_ROW + 16 * q, eb = smb + A_EB + w * KG_EB + r * KG_ROW + 16 * q;
    v4u a[2][4], bb[2][4];
    sfor<0, 4>([&](auto S) {
      constexpr int s = S;
      a_dsr128<64 * s>(a[0][s], ga); a_dsr128<64 * s + 16 * KG_ROW>(a[1][s], ga);
      a_dsr128<64 * s>(bb[0][s], eb); a_dsr128<64 * s + 16 * KG_ROW>(bb[1][s], eb);
    });
    a_lgkm0();
#pragma unroll
    for (int s = 0; s < 4; ++s)
#pragma unroll
      for (int i = 0; i < 4; ++i) a_mfma_h(d[i >> 1][i & 1], a[i >> 1][s], bb[i & 1][s]);
    asm volatile("s_nop 15" ::: "memory");
#pragma unroll
    for (int i = 0; i < 4; ++i) a_pin(d[i >> 1][i & 1]);
    LAS char* px = sm + A_PX + w * 2048 + 16 * lane;
    if (tid == 128 * (w >> 1)) *(LAS float*)(sm + A_SC + 4 * (w >> 1)) = kscale;
    if (hf == 0) {
      *(LAS f32x4*)px = d[0][1]; *(LAS f32x4*)(px + 1024) = d[1][1]; d0 = d[0][0]; d1 = d[1][0];
    } else {
      *(LAS f32x4*)px = d[0][0]; *(LAS f32x4*)(px + 1024) = d[1][0]; d0 = d[0][1]; d1 = d[1][1];
    }
  }
  SC(stp, 2);
  BAR();
  {
    const int r = lane & 15, q = lane >> 4;
    LAS char* po = sm + A_PX + (w ^ 1) * 2048 + 16 * lane;
    d0 += *(LAS f32x4*)po;
    d1 += *(LAS f32x4*)(po + 1024);
    const int j = 16 * hf + r;
    LAS char* T = sm + A_T + (w >> 1) * T_CH;
    const int ib = q < 2 ? 4 * q + 8 * j : (q == 2 ? 256 + 4 * j : 384 + 2 * j);
    const int is2 = q == 3 ? 64 : 2;
    *(LAS unsigned*)(T + 2 * ib) = pkh(d0[0], d0[1]);
    *(LAS unsigned*)(T + 2 * (ib + is2)) = pkh(d0[2], d0[3]);
#pragma unroll
    for (int i = 0; i < 4; ++i) {
      const int i1 = q == 0 ? 512 + 32 * i + j : ((q == 1 && i == 0 && j == 0) ? 640 : 641 + i);
      *(LAS _Float16*)(T + 2 * i1) = (_Float16)d1[i];
    }
    if (hf == 0) {
      const unsigned k01 = __builtin_amdgcn_readfirstlane(pkh(d0[0], d0[1])), k23 = __builtin_amdgcn_readfirstlane(pkh(d0[2], d0[3]));
      const int l = q - r;
      const unsigned kk = l >= 2 ? k23 : k01;
      const unsigned val = l < 0 ? 0u : ((l & 1) ? kk >> 16 : kk);
      *(LAS u16*)(sm + A_KG + (w >> 1) * KC_CH + q * KC_COPY + 2 * (2048 + r)) = (u16)val;
    }
    if (tid >= 384) {
      const v4u z = {0, 0, 0, 0};
      *(LAS v4u*)(sm + A_U + ((tid - 384) >> 5) * U_CH + 16 * (tid & 31)) = z;
    }
  }
  SC(stp, 3);
  BAR();
  const float kscale_ch = *(LAS float*)(sm + A_SC + 4 * (w & 3));
  v4u sg[2];
  g_ld128_sc1<0>(sg[0], 32 * tid, p.ws + WS_STATS);
  g_ld128_sc1<16>(sg[1], 32 * tid, p.ws + WS_STATS);
  {
    const unsigned eo[11] = {et[0].x, et[0].y, et[0].z, et[0].w, et[1].x, et[1].y, et[1].z, et[1].w, et[2].x, et[2].y, et[2].z};
    unsigned v[2][11];
    if (epair == 0) {
      sfor<0, 11>([&](auto I) { a_dsr16<A_T + 0 * T_CH>(v[0][decltype(I)::value], eo[decltype(I)::value]); });
      sfor<0, 11>([&](auto I) { a_dsr16<A_T + 1 * T_CH>(v[1][decltype(I)::value], eo[decltype(I)::value]); });
    } else {
      sfor<0, 11>([&](auto I) { a_dsr16<A_T + 2 * T_CH>(v[0][decltype(I)::value], eo[decltype(I)::value]); });
      sfor<0, 11>([&](auto I) { a_dsr16<A_T + 3 * T_CH>(v[1][decltype(I)::value], eo[decltype(I)::value]); });
    }
    sfor<0, 2>([&](auto C) {
      constexpr int cc = C;
      if constexpr (cc == 0) a_waitl<11>(); else a_waitl<4>();
#pragma unroll
      for (int i = 0; i < 11; ++i) a_pin(v[cc][i]);
      unsigned d[2][6];
#pragma unroll
      for (int i = 0; i < 5; ++i) d[cc][i] = v[cc][2 * i] | (v[cc][2 * i + 1] << 16);
      d[cc][5] = v[cc][10];
      LAS char* K = sm + A_KG + (2 * epair + cc) * KC_CH + 16 * ec;
      v4u c3 = {d[cc][0], d[cc][1], d[cc][2], d[cc][3]};
      v4u c1 = {d[cc][1], d[cc][2], d[cc][3], d[cc][4]};
      v4u c2, c0v;
#pragma unroll
      for (int i = 0; i < 4; ++i) {
        c2[i] = __builtin_amdgcn_alignbit(d[cc][i + 1], d[cc][i], 16);
        c0v[i] = __builtin_amdgcn_alignbit(d[cc][i + 2], d[cc][i + 1], 16);
      }
      a_dsw128<0 * KC_COPY>(lds_addr(K), c0v);
      a_dsw128<1 * KC_COPY>(lds_addr(K), c1);
      a_dsw128<2 * KC_COPY>(lds_addr(K), c2);
      a_dsw128<3 * KC_COPY>(lds_addr(K), c3);
    });
  }
  SC(stp, 4);
  {
    asm volatile("s_waitcnt vmcnt(0)" : "+v"(sg[0]), "+v"(sg[1]), "+v"(xg[0]), "+v"(xg[1]), "+v"(xg[2]), "+v"(xg[3])::"memory");
    for (unsigned spin = 0; spin < (1u << 20); ++spin) {
      if (__all(sg[0].y == tag && sg[0].w == tag && sg[1].y == tag && sg[1].w == tag)) break;
      __builtin_amdgcn_s_sleep(1);
      g_ld128_sc1<0>(sg[0], 32 * tid, p.ws + WS_STATS);
      g_ld128_sc1<16>(sg[1], 32 * tid, p.ws + WS_STATS);
      asm volatile("s_waitcnt vmcnt(0)" : "+v"(sg[0]), "+v"(sg[1])::"memory");
    }
    SC(stp, 5);
    f32x2 ua[4], ub[4];
#pragma unroll
    for (int k = 0; k < 4; ++k) {
      const unsigned st = k == 0 ? sg[0].x : k == 1 ? sg[0].z : k == 2 ? sg[1].x : sg[1].z;
      const float rs = __builtin_amdgcn_rcpf(bfhi(st)), mr = -bflo(st) * rs;
      ua[k] = fma2(unbf(xg[k].x), (f32x2){rs, rs}, (f32x2){mr, mr});
      ub[k] = fma2(unbf(xg[k].y), (f32x2){rs, rs}, (f32x2){mr, mr});
    }
    LAS char* ud = sm + A_U + 512 + 8 * tid;
    *(LAS v2u*)(ud + 0 * U_CH) = (v2u){pkh(ua[0].x, ua[1].x), pkh(ua[2].x, ua[3].x)};
    *(LAS v2u*)(ud + 1 * U_CH) = (v2u){pkh(ua[0].y, ua[1].y), pkh(ua[2].y, ua[3].y)};
    *(LAS v2u*)(ud + 2 * U_CH) = (v2u){pkh(ub[0].x, ub[1].x), pkh(ub[2].x, ub[3].x)};
    *(LAS v2u*)(ud + 3 * U_CH) = (v2u){pkh(ub[0].y, ub[1].y), pkh(ub[2].y, ub[3].y)};
    const unsigned s0 = 0x05040100u, s1 = 0x07060302u;
    LAS char* xd = sm + A_X + 8 * tid;
    *(LAS v2u*)(xd + 0 * 4096) = (v2u){__builtin_amdgcn_perm(xg[1].x, xg[0].x, s0), __builtin_amdgcn_perm(xg[3].x, xg[2].x, s0)};
    *(LAS v2u*)(xd + 1 * 4096) = (v2u){__builtin_amdgcn_perm(xg[1].x, xg[0].x, s1), __builtin_amdgcn_perm(xg[3].x, xg[2].x, s1)};
    *(LAS v2u*)(xd + 2 * 4096) = (v2u){__builtin_amdgcn_perm(xg[1].y, xg[0].y, s0), __builtin_amdgcn_perm(xg[3].y, xg[2].y, s0)};
    *(LAS v2u*)(xd + 3 * 4096) = (v2u){__builtin_amdgcn_perm(xg[1].y, xg[0].y, s1), __builtin_amdgcn_perm(xg[3].y, xg[2].y, s1)};
  }
  SC(stp, 6);
  BAR();
  if (w < 4) {
    const int ch = w, r = lane & 15, q = lane >> 4;
    f32x4 acc[8];
#pragma unroll
    for (int i = 0; i < 8; ++i) { acc[i] = (f32x4){0, 0, 0, 0}; a_pin(acc[i]); }
    unsigned aA = smb + A_KG + ch * KC_CH + (r & 3) * KC_COPY + 2 * (2048 - 16 - 4 * (r >> 2) + 8 * q) - 512 * 7 - 64;
    unsigned aB = smb + A_U + ch * U_CH + 512 + 32 * r - 32 + 16 * q - 64;
    ConvSet R0, R1;
    auto rd = [&](auto J, auto OFFc, ConvSet& R, unsigned cA, unsigned cB) {
      constexpr int j = J, off = OFFc;
      if constexpr (j >= 2 && j < 10) a_dsr128<512 * (j - 2) + off>(R.b[j - 2], cB);
      else {
        constexpr int jj = j < 2 ? j : j - 8, g = jj >> 1;
        if constexpr (jj & 1) a_dsr64<512 * (7 - g) + off + 8>(R.ah[g], cA);
        else a_dsr64<512 * (7 - g) + off>(R.al[g], cA);
      }
    };
    v2u uv[8], xv[8];
    const unsigned up = smb + A_U + ch * U_CH + 512 + 32 * r + 8 * q, xp = smb + A_X + ch * 4096 + 32 * r + 8 * q;
    auto block = [&](auto MODEc, auto OFFc, ConvSet& X, ConvSet& Y, unsigned cA, unsigned cB) {
      constexpr int mode = MODEc;
      sfor<0, 8>([&](auto G) {
        constexpr int g = G, m0 = g * 8 - g * (g - 1) / 2;
        const v4u a = {X.al[g].x, X.al[g].y, X.ah[g].x, X.ah[g].y};
        sfor<g, 8>([&](auto N) {
          constexpr int nn = N, m = m0 + nn - g;
          if constexpr (mode == 0) {
            constexpr int need = g == 0 ? 2 + nn : 9 + 2 * g, mine = m < 24 ? m : 24, out = 23 - need + mine;
            if constexpr (g == 0 ? nn == 7 : nn == g) a_waitl<(out < 15 ? out : 15)>();
          }
          a_mfma_h(acc[nn], a, X.b[nn - g]);
          if constexpr (mode < 2 && m < 24) rd(std::integral_constant<int, m>{}, OFFc, Y, cA, cB);
          if constexpr (mode == 2 && m < 16) {
            if constexpr (m & 1) a_dsr64<512 * (m >> 1)>(xv[m >> 1], xp); else a_dsr64<512 * (m >> 1)>(uv[m >> 1], up);
          }
        });
      });
      a_lgkm0();
    };
    using M0 = std::integral_constant<int, 0>; using M1 = std::integral_constant<int, 1>; using M2 = std::integral_constant<int, 2>;
    using OE = std::integral_constant<int, 64>; using OO = std::integral_constant<int, 0>;
    sfor<0, 24>([&](auto J) { rd(J, OE{}, R0, aA, aB); });
    a_waitl<15>();
    block(M0{}, OO{}, R0, R1, aA, aB);
    aA -= 128; aB -= 128;
    block(M1{}, OE{}, R1, R0, aA, aB);
    for (int it = 0; it < 2; ++it) {
      block(M1{}, OO{}, R0, R1, aA, aB);
      aA -= 128; aB -= 128;
      block(M1{}, OE{}, R1, R0, aA, aB);
    }
    block(M1{}, OO{}, R0, R1, aA, aB);
    block(M2{}, OO{}, R1, R0, aA, aB);
    asm volatile("s_nop 15" ::: "memory");
#pragma unroll
    for (int i = 0; i < 8; ++i) { a_pin(acc[i]); a_pin(uv[i]); a_pin(xv[i]); }
    SC(stp, 7);
    const f32x2 sc2 = {kscale_ch, kscale_ch};
    const float dsk = bf2f(vdsk);
    v2u ho[8];
#pragma unroll
    for (int i = 0; i < 8; ++i) {
      const f32x2 b0 = {__builtin_fmaf(hlo(uv[i].x), dsk, bflo(xv[i].x)), __builtin_fmaf(hhi(uv[i].x), dsk, bfhi(xv[i].x))};
      const f32x2 b1 = {__builtin_fmaf(hlo(uv[i].y), dsk, bflo(xv[i].y)), __builtin_fmaf(hhi(uv[i].y), dsk, bfhi(xv[i].y))};
      const f32x2 h0 = fma2((f32x2){acc[i][0], acc[i][1]}, sc2, b0), h1 = fma2((f32x2){acc[i][2], acc[i][3]}, sc2, b1);
      ho[i] = (v2u){pkbf(h0.x, h0.y), pkbf(h1.x, h1.y)};
    }
    SC(stp, 8);
    const unsigned ho_ = (unsigned)WS_HT + 2 * ((c0 + ch) * L + 256 * (q & 1) + 16 * r + 8 * (q >> 1));
#pragma unroll
    for (int i = 0; i < 8; i += 2) {
      auto sx = __builtin_amdgcn_permlane16_swap(ho[i].x, ho[i + 1].x, false, false);
      auto sy = __builtin_amdgcn_permlane16_swap(ho[i].y, ho[i + 1].y, false, false);
      ws_st128(p, ho_ + 512 * i, (v4u){(unsigned)sx[0], (unsigned)sy[0], (unsigned)sx[1], (unsigned)sy[1]});
    }
  }
  SC(stp, 9);
  return tag;
}

DI void helper_role(const Params& p, LAS char* sm, int lane, int w, int hidx) {
  const int g = lane >> 4, qq = (lane & 15) >> 2, pp = lane & 3;
  if (hidx < 54) {
    const int mat = hidx / 27, cb = hidx % 27;
    const u16* W = mat ? p.w3 : p.w1;
    LAS char* buf = sm + w * 12288;
#pragma unroll
    for (int i = 0; i < 3; ++i) {
      const int s = w + 8 * i;
      if (s < 20) {
#pragma unroll
        for (int k = 0; k < 4; ++k)
          glds16(W + (size_t)(32 * s + 8 * k + (lane >> 3)) * F + 64 * cb + 8 * (lane & 7), buf + i * 4096 + k * 1024);
      }
    }
    WAITV(0);
    float cs[4] = {0, 0, 0, 0};
#pragma unroll
    for (int i = 0; i < 3; ++i) {
      const int s = w + 8 * i;
      if (s < 20) {
#pragma unroll
        for (int u = 0; u < 4; ++u) {
          LAS char* a = buf + i * 4096 + (8 * g + qq) * 128 + u * 32 + pp * 8;
          const v2u lo = trread(a), hi = trread(a + 4 * 128);
          const v4u fr = {lo.x, lo.y, hi.x, hi.y};
          const int un = 4 * cb + u, cgu = un < 84 ? un / 7 : 12 + (un - 84) / 6, ul = un < 84 ? un % 7 : (un - 84) % 6;
          ws_st128(p, (unsigned)WS_W13 + ((cgu * 10 + (s >> 1)) * 28 + 4 * ul + 2 * mat + (s & 1)) * 1024 + 16 * lane, fr);
          cs[u] += (bflo(fr.x) + bfhi(fr.x)) + (bflo(fr.y) + bfhi(fr.y)) + (bflo(fr.z) + bfhi(fr.z)) + (bflo(fr.w) + bfhi(fr.w));
        }
      }
    }
    LAS float* red = (LAS float*)(sm + 98304);
#pragma unroll
    for (int u = 0; u < 4; ++u) {
      const float t = xrow16(cs[u]);
      if (lane < 16) red[w * 64 + u * 16 + lane] = t;
    }
    __syncthreads();
    if (w == 0) {
      float t = 0;
#pragma unroll
      for (int i = 0; i < 8; ++i) t += red[i * 64 + lane];
      ws_st(p, WS_CS + 4 * (mat * F + 64 * cb + lane), __builtin_bit_cast(unsigned, t));
    }
  } else {
    LAS char* buf = sm + w * 8192;
#pragma unroll
    for (int i = 0; i < 2; ++i) {
      const int blk = (hidx - 54) * 8 + w + 336 * i;
      if (blk < 540) {
        const int s = blk / 10, cb = blk % 10;
#pragma unroll
        for (int k = 0; k < 4; ++k)
          glds16(p.w2 + (size_t)(32 * s + 8 * k + (lane >> 3)) * D + 64 * cb + 8 * (lane & 7), buf + i * 4096 + k * 1024);
      }
    }
    WAITV(0);
#pragma unroll
    for (int i = 0; i < 2; ++i) {
      const int blk = (hidx - 54) * 8 + w + 336 * i;
      if (blk < 540) {
        const int s = blk / 10, cb = blk % 10;
#pragma unroll
        for (int u = 0; u < 4; ++u) {
          LAS char* a = buf + i * 4096 + (8 * g + qq) * 128 + u * 32 + pp * 8;
          const v2u lo = trread(a), hi = trread(a + 4 * 128);
          const v4u fr = {lo.x, lo.y, hi.x, hi.y};
          const int dt = 4 * cb + u;
          ws_st128(p, (unsigned)WS_W2 + (((dt / 5) * 18 + s / 3) * 15 + (dt % 5) * 3 + s % 3) * 1024 + 16 * lane, fr);
        }
      }
    }
  }
}

#define X_BLK1 1
constexpr int G1_STAGE = 16384 + 28 * 1024;
constexpr int G1_RED = 148480;
constexpr int G1_CS = G1_RED + 1024;
constexpr int G1_LDS = G1_CS + 1024;
static_assert(3 * G1_STAGE <= G1_RED, "lds map");

template <int UH> struct G1Set {
  v4u a[UH ? 6 : 8];
  v2u b[2][2];
};

template <int UH, class Hook>
DI void g1_wave(const Params& p, LAS char* sm, int lane, int tw, int rg, int cg, int u0, int nu, int kt0, Stamps& stp, Hook&& after_loop) {
  constexpr int NU = UH ? 3 : 4, UB = UH ? 4 : 0, NA = 2 * NU, NR = NA + 4, NM = 2 * NA, ND = UH ? 7 : 4;
  const unsigned smb = lds_addr(sm);
  const int g = lane >> 4, qq = (lane & 15) >> 2, pp = lane & 3, r = lane & 15;
  unsigned voff[UH ? 1 : 4];
  const unsigned char* gb;
  constexpr unsigned KSTR = UH ? 28 * 1024 : 64 * L * 2;
  unsigned m0s;
  if constexpr (UH == 0) {
#pragma unroll
    for (int i = 0; i < 4; ++i) {
      const int row = 16 * tw + 4 * i + (lane >> 4);
      const int ch = (lane & 15) ^ (((row & 3) << 2) | ((row >> 2) & 3));
      voff[i] = (unsigned)(row * L * 2 + ch * 16 - 1024 * i);
    }
    gb = p.ws + WS_HT + 256 * rg;
    m0s = smb + 4096 * tw;
  } else {
    voff[0] = 16 * lane;
    gb = p.ws + WS_W13 + ((size_t)cg * 280 + 7 * tw + 3) * 1024;
    m0s = smb + 16384 + (7 * tw + 3) * 1024;
  }
  auto dma = [&](auto I, const unsigned char* base) {
    constexpr int i = I;
    if constexpr (UH == 0) a_dma_o<1024 * i>(voff[i], base);
    else a_dma_o<1024 * (i - 3)>(voff[0], base);
  };
  auto kbase = [&](int it) {
    const int blk = it / X_BLK1;
    int k = it - blk * X_BLK1 + kt0; k = k >= X_BLK1 ? k - X_BLK1 : k;
    return gb + (size_t)((unsigned)(blk * X_BLK1 + k) * KSTR);
  };
  const unsigned aA = smb + 16384 + 16 * lane;
  unsigned aB[2][2];
#pragma unroll
  for (int t = 0; t < 2; ++t)
#pragma unroll
    for (int tt = 0; tt < 2; ++tt) {
      const int row = 8 * g + 4 * tt + qq;
      const int ch = (2 * (2 * tw + t) + (pp >> 1)) ^ (((row & 3) << 2) | ((row >> 2) & 3));
      aB[t][tt] = smb + row * 256 + 16 * ch + 8 * (pp & 1);
    }
  f32x4 acc[NA][2];
#pragma unroll
  for (int i = 0; i < NA; ++i) {
    acc[i][0] = (f32x4){0, 0, 0, 0}; acc[i][1] = (f32x4){0, 0, 0, 0};
    a_pin(acc[i][0]); a_pin(acc[i][1]);
  }
  float ssum = 0, ssq = 0;
  a_pin(ssum); a_pin(ssq);
  G1Set<UH> R0, R1;

  auto rd = [&](auto J, auto KS, G1Set<UH>& R, unsigned cA, const unsigned (&cB)[2][2]) {
    constexpr int j = J, ks = KS;
    if constexpr (j < 4) a_dstr<ks * 8192>(R.b[j >> 1][j & 1], cB[j >> 1][j & 1]);
    else { constexpr int a = j - 4; a_dsr128<(4 * (UB + (a >> 1)) + 2 * (a & 1) + ks) * 1024>(R.a[a], cA); }
  };
  auto mm = [&](auto M, G1Set<UH>& R) {
    constexpr int m = M, a = m >> 1, t = m & 1;
    const v4u bb = {R.b[t][0].x, R.b[t][0].y, R.b[t][1].x, R.b[t][1].y};
    a_mfma(acc[a][t], R.a[a], bb);
  };
  auto stat = [&](auto S, G1Set<UH>& R) {
    constexpr int s = S, e = s >> 1;
    const unsigned hw = e == 0 ? R.b[UH][0].x : e == 1 ? R.b[UH][0].y : e == 2 ? R.b[UH][1].x : R.b[UH][1].y;
    a_stat<s & 1>(ssum, ssq, hw);
  };
  auto block = [&](auto KS, auto RGc, auto CNT, auto DO_DMA, G1Set<UH>& X, G1Set<UH>& Y, unsigned cA, const unsigned (&cB)[2][2],
                   const unsigned char* base, unsigned m0v) {
    constexpr int RG = RGc;
    if constexpr (DO_DMA) a_m0(m0v);
    sfor<0, NM>([&](auto M) {
      constexpr int m = M;
      constexpr int issued = RG == 0 ? 0 : (m * NR + RG - 1) / RG < NR ? (m * NR + RG - 1) / RG : NR;
      if constexpr (CNT && (m & 1) == 0) a_waitl<(NR - 1 - (4 + m / 2)) + issued>();
      mm(M, X);
      if constexpr (RG > 0) {
        constexpr int nxt = ((m + 1) * NR + RG - 1) / RG < NR ? ((m + 1) * NR + RG - 1) / RG : NR;
        sfor<issued, nxt>([&](auto J) { rd(J, KS, Y, cA, cB); });
      }
      if constexpr (DO_DMA) {
        sfor<0, ND>([&](auto Dd) { if constexpr ((decltype(Dd)::value * NM + NM / 2) / ND == m) dma(Dd, base); });
      }
      sfor<0, 8>([&](auto S) { if constexpr ((decltype(S)::value * NM + NM / 2) / 8 == m) stat(S, X); });
    });
  };
  using T1 = std::true_type; using T0 = std::false_type;
  using K0 = std::integral_constant<int, 0>; using K1 = std::integral_constant<int, 1>;
#define X_RGB 4
  using RGA = std::integral_constant<int, NM>;
  using RGB = std::integral_constant<int, NM - X_RGB>;

  if (UH == 1 && tw == 0) {
    const unsigned co = lane < 28 ? 64 * u0 + 16 * lane : 4 * F + 64 * u0 + 16 * ((lane < 56 ? lane : 55) - 28);
    a_dma(smb + G1_CS, co, p.ws + WS_CS);
  }
  a_m0(m0s);
  { const unsigned char* b0 = kbase(0); asm volatile("s_nop 0"); sfor<0, ND>([&](auto I) { dma(I, b0); }); }
  a_m0(m0s + G1_STAGE);
  { const unsigned char* b1 = kbase(1); asm volatile("s_nop 0"); sfor<0, ND>([&](auto I) { dma(I, b1); }); }
  a_waitv<ND>();
  a_bar();
  SC(stp, 13);
  {
    unsigned cB[2][2] = {{aB[0][0], aB[0][1]}, {aB[1][0], aB[1][1]}};
    sfor<0, NR>([&](auto J) { rd(J, K0{}, R0, aA, cB); });
    a_m0(m0s + 2 * G1_STAGE);
    { const unsigned char* b2 = kbase(2); asm volatile("s_nop 0"); sfor<0, ND>([&](auto I) { dma(I, b2); }); }
    block(K1{}, RGB{}, T1{}, T0{}, R0, R1, aA, cB, nullptr, 0);
  }
  unsigned so = 0;
  auto step = [&](int it, auto DO_DMA, auto NW) {
    const unsigned sn = so;
    so += G1_STAGE; so = so == 3 * G1_STAGE ? 0 : so;
    const unsigned cA = aA + so;
    const unsigned cB[2][2] = {{aB[0][0] + so, aB[0][1] + so}, {aB[1][0] + so, aB[1][1] + so}};
    const unsigned char* base = kbase(it + 2);
    a_waitv<decltype(NW)::value>();
    a_lgkm0();
    a_bar();
    block(K0{}, RGA{}, T0{}, DO_DMA, R1, R0, cA, cB, base, m0s + sn);
    block(K1{}, RGB{}, T1{}, T0{}, R0, R1, cA, cB, nullptr, 0);
  };
  for (int it = 1; it < 8; ++it) step(it, T1{}, std::integral_constant<int, ND>{});
  step(8, T0{}, std::integral_constant<int, ND>{});
  step(9, T0{}, std::integral_constant<int, 0>{});
  LAS float* red = (LAS float*)(sm + G1_RED);
  a_lgkm0();
  sfor<0, 8>([&](auto S) { stat(S, R1); });
  sfor<0, NM / 2>([&](auto M) { mm(M, R1); });
  {
    const float mean = xrow16(ssum) * (1.0f / D);
    const float var = fmaxf(xrow16(ssq) * (1.0f / D) - mean * mean, 0.f);
    const float sd = rbf(__builtin_amdgcn_sqrtf(rbf(rbf(var) + 1e-5f)));
    if (g == 0) *(LAS f32x2*)(red + 2 * (32 * tw + 16 * UH + r)) = (f32x2){rbf(mean), __builtin_amdgcn_rcpf(sd)};
  }
  sfor<NM / 2, NM>([&](auto M) { mm(M, R1); });
  SC(stp, 14);
  BAR();
  f32x4 c1[NU], c3[NU];
#pragma unroll
  for (int i = 0; i < NU; ++i) {
    c1[i] = *(LAS f32x4*)(sm + G1_CS + 64 * (UB + i) + 16 * g);
    c3[i] = *(LAS f32x4*)(sm + G1_CS + 448 + 64 * (UB + i) + 16 * g);
  }
  f32x2 st[2];
#pragma unroll
  for (int t = 0; t < 2; ++t) st[t] = *(LAS f32x2*)(red + 2 * (32 * tw + 16 * t + r));
  asm volatile("s_nop 15" ::: "memory");
#pragma unroll
  for (int i = 0; i < NA; ++i) { a_pin(acc[i][0]); a_pin(acc[i][1]); }
  SC(stp, 15);
#pragma unroll
  for (int i = 0; i < NU; ++i) {
    after_loop(i);
    if (UB + i < nu) {
      const int un = u0 + UB + i;
      v2u o[2];
#pragma unroll
      for (int t = 0; t < 2; ++t) {
        const f32x2 nm = {-st[t].x, -st[t].x};
        const float ra1 = st[t].y * -1.4426950408889634f, r21 = st[t].y * st[t].y;
        const f32x2 ra = {ra1, ra1}, r2 = {r21, r21}, one = {1.f, 1.f};
        unsigned pk[2];
#pragma unroll
        for (int e = 0; e < 2; ++e) {
          const f32x2 cg2 = {c1[i][2 * e], c1[i][2 * e + 1]}, cu2 = {c3[i][2 * e], c3[i][2 * e + 1]};
          const f32x2 a = fma2(nm, cg2, (f32x2){acc[2 * i][t][2 * e], acc[2 * i][t][2 * e + 1]});
          const f32x2 bq = fma2(nm, cu2, (f32x2){acc[2 * i + 1][t][2 * e], acc[2 * i + 1][t][2 * e + 1]});
          const f32x2 arg = a * ra;
          const f32x2 den = one + (f32x2){__builtin_amdgcn_exp2f(arg.x), __builtin_amdgcn_exp2f(arg.y)};
          const f32x2 sg = {__builtin_amdgcn_rcpf(den.x), __builtin_amdgcn_rcpf(den.y)};
          const f32x2 hv = (a * bq) * (sg * r2);
          pk[e] = pkbf(hv.x, hv.y);
        }
        o[t] = (v2u){pk[0], pk[1]};
      }
      auto sx = __builtin_amdgcn_permlane16_swap(o[0].x, o[1].x, false, false);
      auto sy = __builtin_amdgcn_permlane16_swap(o[0].y, o[1].y, false, false);
      const v4u ov = {(unsigned)sx[0], (unsigned)sy[0], (unsigned)sx[1], (unsigned)sy[1]};
      const int ttile = 8 * rg + 2 * tw + (g & 1), chunk = 2 * (un & 1) + (g >> 1);
      ws_st128(p, (unsigned)WS_HM + (ttile * 54 + (un >> 1)) * 1024 + chunk * 256 + 16 * r, ov);
    }
  }
  SC(stp, 16);
}

#define X_BLK2 6
constexpr int G2_KS = 3;
constexpr int G2_NT = 54 / G2_KS;
constexpr int G2_STAGE = 9 * G2_KS * 1024;
constexpr int G2_NST = 5;
constexpr int G2_H = G2_NST * G2_STAGE;
constexpr int G2_LDS = G2_H + 80 * 128;

template <int DH> struct G2Set {
  v4u b[G2_KS];
  v4u a[DH ? 2 : 3][G2_KS];
};

template <int DH, int NDMA>
DI void g2_dma_tile(const Params& p, unsigned smb, int lane, int tt, int t0, int dg, int kt0, int it, unsigned so) {
  constexpr int KS = G2_KS;
  const unsigned char* gb = DH ? p.ws + WS_W2 + ((size_t)dg * 18 * 15 + 4 * tt) * 1024 : p.ws + WS_HM + (size_t)((t0 >> 4) + tt) * 54 * 1024;
  constexpr unsigned KSTR = DH ? 15 * 1024 : KS * 1024;
  const int blk = it / X_BLK2;
  int k = it - blk * X_BLK2 + kt0; k = k >= X_BLK2 ? k - X_BLK2 : k;
  const unsigned char* base = gb + (size_t)((unsigned)(blk * X_BLK2 + k) * KSTR);
  a_m0n(smb + (DH ? 4 * KS + 4 * tt : KS * tt) * 1024 + so);
  sfor<0, NDMA>([&](auto I) { a_dma_o<1024 * decltype(I)::value>(16 * lane, base); });
}
template <int DH, int NDMA>
DI void g2_prefetch(const Params& p, LAS char* sm, int lane, int tt, int t0, int d0, int dg, int kt0, int part) {
  const unsigned smb = lds_addr(sm);
  if constexpr (DH == 0) {
    const unsigned char* hb = p.ws + WS_HT + ((size_t)d0 * L + t0) * 2;
    const unsigned ho = (unsigned)((lane >> 3) * L * 2 + (lane & 7) * 16);
    const int piece = tt + 4 * part;
    if (piece < 10) a_dma(smb + G2_H + piece * 1024, ho, hb + (size_t)piece * 8 * L * 2);
  } else {
    g2_dma_tile<1, NDMA>(p, smb, lane, tt, t0, dg, kt0, part, part * G2_STAGE);
    if (part == 2) g2_dma_tile<1, NDMA>(p, smb, lane, tt, t0, dg, kt0, 3, 3 * G2_STAGE);
  }
}

template <int DH, int NDMA>
DI void g2_wave(const Params& p, LAS char* sm, int lane, int tt, int t0, int d0, int dg, int kt0, int rg, unsigned tag, Stamps& stp) {
  constexpr int ND = DH ? 2 : 3, DB = DH ? 3 : 0, NR = G2_KS * (1 + ND), NM = G2_KS * ND, KS = G2_KS;
  const unsigned smb = lds_addr(sm);
  auto dma_tile = [&](int it, unsigned so) { g2_dma_tile<DH, NDMA>(p, smb, lane, tt, t0, dg, kt0, it, so); };
  const unsigned aB = smb + KS * tt * 1024 + 16 * lane, aA = smb + (4 * KS + DB * KS) * 1024 + 16 * lane;
  f32x4 acc[ND];
#pragma unroll
  for (int i = 0; i < ND; ++i) { acc[i] = (f32x4){0, 0, 0, 0}; a_pin(acc[i]); }
  G2Set<DH> R0, R1;
  auto rd = [&](auto J, G2Set<DH>& R, unsigned so) {
    constexpr int j = J;
    if constexpr (j < KS) a_dsr128<j * 1024>(R.b[j], aB + so);
    else { constexpr int a = (j - KS) / KS, ks = (j - KS) % KS; a_dsr128<(a * KS + ks) * 1024>(R.a[a][ks], aA + so); }
  };
  auto mm = [&](auto M, G2Set<DH>& R) {
    constexpr int m = M, ks = m / ND, a = m % ND;
    a_mfma(acc[a], R.a[a][ks], R.b[ks]);
  };
  group_wait(p, rg, lane, DH == 0 ? tt : 4, tag);
  SC(stp, 18);
  if constexpr (DH == 0) {
#pragma unroll
    for (int i = 0; i < G2_NST - 1; ++i) dma_tile(i, i * G2_STAGE);
  }
  auto waitv = [&](auto N) { a_waitv<decltype(N)::value>(); };
  auto step = [&](int it, unsigned so, unsigned sn, auto FIRST, auto DODMA, G2Set<DH>& X, G2Set<DH>& Y) {
    a_lgkm0();
    a_bar();
    if constexpr (FIRST) {
      sfor<0, NR>([&](auto J) { rd(J, Y, so); });
      if constexpr (DODMA) dma_tile(it + G2_NST - 1, sn);
    } else {
      sfor<0, NM>([&](auto M) {
        constexpr int m = M;
        mm(M, X);
        if constexpr (2 * m < NR) rd(std::integral_constant<int, 2 * m>{}, Y, so);
        if constexpr (2 * m + 1 < NR) rd(std::integral_constant<int, 2 * m + 1>{}, Y, so);
        if constexpr (m == NM - 1) {
          sfor<(2 * NM < NR ? 2 * NM : NR), NR>([&](auto J) { rd(J, Y, so); });
          if constexpr (DODMA) dma_tile(it + G2_NST - 1, sn);
        }
      });
    }
  };
  using T1 = std::true_type; using T0 = std::false_type;
  constexpr int INF = (G2_NST - 2) * NDMA;
  waitv(std::integral_constant<int, INF>{});
  SC(stp, 19);
  step(0, 0, (G2_NST - 1) * G2_STAGE, T1{}, T1{}, R1, R0);
  unsigned so = 0;
  auto nxt = [&](unsigned s) { s += G2_STAGE; return s == G2_NST * G2_STAGE ? 0u : s; };
  auto prv = [&](unsigned s) { return s == 0 ? (unsigned)((G2_NST - 1) * G2_STAGE) : s - G2_STAGE; };
  constexpr int NSTEADY = G2_NT - G2_NST;
  for (int it = 1; it + 1 <= NSTEADY; it += 2) {
    so = nxt(so);
    waitv(std::integral_constant<int, INF>{});
    step(it, so, prv(so), T0{}, T1{}, R0, R1);
    so = nxt(so);
    waitv(std::integral_constant<int, INF>{});
    step(it + 1, so, prv(so), T0{}, T1{}, R1, R0);
  }
  static_assert(NSTEADY == 13 && G2_NST == 5, "tail below is written for this shape");
  so = nxt(so); waitv(std::integral_constant<int, INF>{});       step(13, so, prv(so), T0{}, T1{}, R0, R1);
  so = nxt(so); waitv(std::integral_constant<int, INF>{});       step(14, so, 0, T0{}, T0{}, R1, R0);
  so = nxt(so); waitv(std::integral_constant<int, 2 * NDMA>{});  step(15, so, 0, T0{}, T0{}, R0, R1);
  so = nxt(so); waitv(std::integral_constant<int, NDMA>{});      step(16, so, 0, T0{}, T0{}, R1, R0);
  so = nxt(so); waitv(std::integral_constant<int, 0>{});         step(17, so, 0, T0{}, T0{}, R0, R1);
  a_lgkm0();
  sfor<0, NM>([&](auto M) { mm(M, R1); });
  asm volatile("s_nop 15\n\ts_nop 3" ::: "memory");
  SC(stp, 20);
  const int r = lane & 15, g = lane >> 4, qq = (lane & 15) >> 2, pp = lane & 3;
  v2u hv[ND];
#pragma unroll
  for (int i = 0; i < ND; ++i) a_dstr<0>(hv[i], smb + G2_H + (16 * (DB + i) + 4 * g + qq) * 128 + 32 * tt + 8 * pp);
  a_lgkm0();
#pragma unroll
  for (int i = 0; i < ND; ++i) a_pin(hv[i]);
#pragma unroll
  for (int i = 0; i < ND; ++i) {
    const float o0 = bflo(hv[i].x) + rbf(acc[i][0]), o1 = bfhi(hv[i].x) + rbf(acc[i][1]);
    const float o2 = bflo(hv[i].y) + rbf(acc[i][2]), o3 = bfhi(hv[i].y) + rbf(acc[i][3]);
    *(GAS v2u*)(p.out + (size_t)(t0 + 16 * tt + r) * D + d0 + 16 * (DB + i) + 4 * g) = (v2u){pkbf(o0, o1), pkbf(o2, o3)};
  }
  SC(stp, 21);
}

constexpr int K_LDS = A_LDS > G1_LDS ? (A_LDS > G2_LDS ? A_LDS : G2_LDS) : (G1_LDS > G2_LDS ? G1_LDS : G2_LDS);
static_assert(K_LDS <= 163840 && K_LDS > 81920, "one workgroup per CU");

__global__ __launch_bounds__(512, 2) void k_block(Params p) {
  extern __shared__ __attribute__((aligned(16))) char smem[];
  LAS char* sm = (LAS char*)smem;
  const int tid = threadIdx.x, lane = tid & 63, w = __builtin_amdgcn_readfirstlane(tid >> 6);
  const int b = blockIdx.x, xl = b & 7, j = b >> 3;
  Stamps stp;
  SR(stp, 30);
  SC(stp, 0);
  unsigned tag;
  if (j < 20) {
    tag = conv_role(p, sm, tid, lane, w, 20 * xl + j, stp);
  } else {
    const int hidx = (j - 20) * 8 + xl;
    tag = ws_ld(p, WS_GEN) + 1u;
    if (hidx < 64) ln_rows(p, 4 * (8 * hidx + w), lane, tag);
    if (hidx == 95 && w == 7) {
      ws_st(p, WS_CNT + lane * 128, 0u);
      ws_st(p, WS_CNT + (64 + lane) * 128, 0u);
      WAITV(0);
      if (lane == 0) ws_st(p, WS_RDY, tag);
    }
    helper_role(p, sm, lane, w, hidx);
    SC(stp, 9);
  }
  tag = __builtin_amdgcn_readfirstlane(tag);
  sync_all(p, b, lane, w, tag, stp);
  SC(stp, 12);
  const int rgi = j & 7, cgi = j >> 3;
  const int rg = 8 * (xl & 1) + rgi, cg = 4 * (xl >> 1) + cgi;
  const int th = (cg >> 2) & 1, dg = 4 * (cg >> 3) + (cg & 3);
  const int t0 = 128 * rg + 64 * th, d0 = 80 * dg;
  const int kt2 = (rgi + cgi) % X_BLK2;
  {
    const int u0 = cg < 12 ? 7 * cg : 84 + 6 * (cg - 12), nu = cg < 12 ? 7 : 6;
    const int kt0 = (rgi + cgi) % X_BLK1;
    if (w < 4) g1_wave<0>(p, sm, lane, w, rg, cg, u0, nu, kt0, stp, [&](int i) { g2_prefetch<0, 3>(p, sm, lane, w, t0, d0, dg, kt2, i); });
    else if (w < 7) g1_wave<1>(p, sm, lane, w - 4, rg, cg, u0, nu, kt0, stp, [&](int i) { g2_prefetch<1, 4>(p, sm, lane, w - 4, t0, d0, dg, kt2, i); });
    else g1_wave<1>(p, sm, lane, 3, rg, cg, u0, nu, kt0, stp, [&](int i) { g2_prefetch<1, 3>(p, sm, lane, 3, t0, d0, dg, kt2, i); });
  }
  group_arrive(p, rg, cg, tid, tag);
  SC(stp, 17);
  if (w < 4) g2_wave<0, 3>(p, sm, lane, w, t0, d0, dg, kt2, rg, tag, stp);
  else if (w < 7) g2_wave<1, 4>(p, sm, lane, w - 4, t0, d0, dg, kt2, rg, tag, stp);
  else g2_wave<1, 3>(p, sm, lane, 3, t0, d0, dg, kt2, rg, tag, stp);
  SR(stp, 31);
  SCW(stp);
}

extern "C" void kernel_launch(void* const* d_in, const int* in_sizes, int n_in, void* d_out, int out_size, void* d_ws,
                              size_t ws_size, hipStream_t stream) {
  Params p;
  p.x = (const u16*)d_in[0];
  p.logA = (const u16*)d_in[1];
  p.Aim = (const u16*)d_in[2];
  p.Cre = (const u16*)d_in[3];
  p.Cim = (const u16*)d_in[4];
  p.logdt = (const u16*)d_in[5];
  p.Dsk = (const u16*)d_in[6];
  p.w1 = (const u16*)d_in[8];
  p.w3 = (const u16*)d_in[9];
  p.w2 = (const u16*)d_in[10];
  p.out = (u16*)d_out;
  p.ws = (unsigned char*)d_ws;
  (void)hipFuncSetAttribute((const void*)k_block, hipFuncAttributeMaxDynamicSharedMemorySize, K_LDS);
  k_block<<<256, 512, K_LDS, stream>>>(p);
}
```
